```python
import torch

def _softmax(x, axis=-1):
    return torch.softmax(x, dim=axis)
def _gelu(x):
    return torch.nn.functional.gelu(x, approximate="tanh")
def _silu(x):
    return torch.nn.functional.silu(x)
def _sigmoid(x):
    return torch.sigmoid(x)
def _layernorm(x, axis=-1, eps=1e-5):
    m = x.mean(dim=axis, keepdim=True)
    v = x.var(dim=axis, keepdim=True, unbiased=False)
    return (x - m) / torch.sqrt(v + eps)
def _scatter_add(tgt, idx, src, axis=0):
    return tgt.clone().index_add_(axis, idx, src)
def _segment_sum(src, seg, n):
    out = torch.zeros((n,) + src.shape[1:], dtype=src.dtype)
    return out.index_add_(0, seg, src)
def _pad(x, pw):
    flat = tuple(v for pair in reversed(pw) for v in pair)
    return torch.nn.functional.pad(x, flat)

kernel_name = 'standalone__gqa_block_cached__near_ridge'

def setup_inputs():

    _w = (lambda a: (lambda t: t.to(torch.bfloat16) if t.is_floating_point() else t)(torch.as_tensor(a)))
    B, Sq, D = 8, 128, 512
    Hq, Hkv, Sk, Dh = 32, 32, 2048, 128
    assert Hq % Hkv == 0, (Hq, Hkv)
    return {
        "x": _w((lambda *s: torch.randn(*s, dtype=torch.bfloat16) * 0.8)(B, Sq, D)),
        "k_cache": _w((lambda *s: torch.randn(*s, dtype=torch.bfloat16) * 0.8)(B, Hkv, Sk, Dh)),
        "v_cache": _w((lambda *s: torch.randn(*s, dtype=torch.bfloat16) * 0.05)(B, Hkv, Sk, Dh)),
        "wq": _w((lambda *s: torch.randn(*s, dtype=torch.bfloat16) * 0.8)(D, Hq * Dh) * (D ** -0.5)),
        "wo": _w((lambda *s: torch.randn(*s, dtype=torch.bfloat16) * 0.05)(Hq * Dh, D) * ((Hq * Dh) ** -0.5)),
    }

def reference(x, k_cache, v_cache, wq, wo):
    B, Sq, D = x.shape
    Hq, Dh = 32, 128
    Hkv = k_cache.shape[1]
    rep = Hq // Hkv
    q = (lambda x, *d: x.permute(*d))((x @ wq).reshape(B, Sq, Hq, Dh), 0, 2, 1, 3)
    k = (lambda x, n, axis: torch.repeat_interleave(x, n, dim=axis))(k_cache, rep, axis=1)
    v = (lambda x, n, axis: torch.repeat_interleave(x, n, dim=axis))(v_cache, rep, axis=1)
    scores = torch.einsum("bhqd,bhkd->bhqk", q, k) * (Dh ** -0.5)
    p = _softmax(scores, axis=-1)
    o = torch.einsum("bhqk,bhkd->bhqd", p, v)
    return (lambda x, *d: x.permute(*d))(o, 0, 2, 1, 3).reshape(B, Sq, Hq * Dh) @ wo
```

```cpp
#include <hip/hip_runtime.h>
#include <hip/hip_bf16.h>
#include <stdint.h>

typedef __attribute__((ext_vector_type(8))) short bf16x8;
typedef __attribute__((ext_vector_type(4))) short s16x4;
typedef __attribute__((ext_vector_type(16))) float f32x16;
typedef __attribute__((ext_vector_type(4))) float f32x4;
typedef __attribute__((ext_vector_type(4))) unsigned u32x4;
typedef __attribute__((ext_vector_type(2))) unsigned u32x2;
typedef __attribute__((address_space(3))) s16x4* lds_s16x4_ptr;
typedef __attribute__((address_space(3))) void* lds_void_ptr;

constexpr int Bn = 8, SQ = 128, DM = 512, NH = 32, SK = 2048, DH = 128;
constexpr int HD = NH * DH;
constexpr int SLOT = 16384;
constexpr int NSLOT = 9;
constexpr int LDS1 = 163840;

typedef __attribute__((ext_vector_type(2))) float f32x2;
typedef __attribute__((ext_vector_type(2))) __bf16 bf16x2_t;
__device__ __forceinline__ unsigned cvtpk(float lo, float hi) {
  f32x2 v = {lo, hi};
  bf16x2_t r = __builtin_convertvector(v, bf16x2_t);
  return __builtin_bit_cast(unsigned, r);
}
__device__ __forceinline__ bf16x8 pack8(float a0, float a1, float a2, float a3, float a4, float a5, float a6, float a7) {
  u32x4 w = {cvtpk(a0, a1), cvtpk(a2, a3), cvtpk(a4, a5), cvtpk(a6, a7)};
  return __builtin_bit_cast(bf16x8, w);
}
__device__ __forceinline__ s16x4 tr_read(const char* p) {
  return __builtin_amdgcn_ds_read_tr16_b64_v4i16((lds_s16x4_ptr)(p));
}
__device__ __forceinline__ bf16x8 cat44(s16x4 a, s16x4 b) {
  return (bf16x8){a[0], a[1], a[2], a[3], b[0], b[1], b[2], b[3]};
}
#define MFMA32(a, b, c) __builtin_amdgcn_mfma_f32_32x32x16_bf16((a), (b), (c), 0, 0, 0)
#define WAIT_VM(n) asm volatile("s_waitcnt vmcnt(" #n ")" ::: "memory")
#define BARRIER() do { asm volatile("s_waitcnt lgkmcnt(0)" ::: "memory"); __builtin_amdgcn_s_barrier(); asm volatile("" ::: "memory"); } while (0)

template <int NT>
__device__ __forceinline__ void dma16(const void* sbase, unsigned voff, unsigned lds) {
  if constexpr (NT)
    asm volatile("s_mov_b32 m0, %2\n\ts_nop 0\n\tglobal_load_lds_dwordx4 %0, %1 nt" :: "v"(voff), "s"(sbase), "s"(lds) : "memory");
  else
    asm volatile("s_mov_b32 m0, %2\n\ts_nop 0\n\tglobal_load_lds_dwordx4 %0, %1" :: "v"(voff), "s"(sbase), "s"(lds) : "memory");
}

__global__ __launch_bounds__(512, 2) void attn_kernel(const __hip_bfloat16* __restrict__ x,
                                                      const __hip_bfloat16* __restrict__ kc,
                                                      const __hip_bfloat16* __restrict__ vc,
                                                      const __hip_bfloat16* __restrict__ wq,
                                                      __hip_bfloat16* __restrict__ ows) {
  extern __shared__ __attribute__((aligned(16))) char shm[];
  const int tid = threadIdx.x;
  const int wid = __builtin_amdgcn_readfirstlane(tid >> 6);
  const int lane = tid & 63;
  const int r = lane & 31, hh = lane >> 5;
  const int qi = wid & 3, kj = wid >> 2;
  const int b = blockIdx.x & 7, head = blockIdx.x >> 3;

  const char* kbase = (const char*)(kc + (size_t)(b * NH + head) * SK * DH);
  const char* vbase = (const char*)(vc + (size_t)(b * NH + head) * SK * DH);

  bf16x8 xf[32];
  {
    const char* xp = (const char*)(x + (size_t)(b * SQ + qi * 32 + r) * DM) + hh * 64;
#pragma unroll
    for (int w = 0; w < 8; ++w) {
#pragma unroll
      for (int s = 0; s < 4; ++s) {
        asm volatile("global_load_dwordx4 %0, %1, off offset:%2" : "=v"(xf[w * 4 + s]) : "v"(xp), "i"(w * 128 + s * 16) : "memory");
      }
    }
  }

  const int drow = lane >> 4, dslot = lane & 15;
  const int krow0 = wid * 8 + drow, krow1 = krow0 + 4;
  const int koff0 = krow0 * 256 + ((dslot ^ (krow0 & 15)) << 4);
  const int koff1 = krow1 * 256 + ((dslot ^ (krow1 & 15)) << 4);
  const int voff0 = krow0 * 256 + ((dslot ^ (drow << 2)) << 4);
  const int voff1 = voff0 + 1024;
  const int woff0 = krow0 * (HD * 2) + ((dslot ^ (drow << 2)) << 4);
  const int woff1 = woff0 + 4 * (HD * 2);
  const char* wbase = (const char*)(wq + head * DH);
  const unsigned ldsw = (unsigned)(uintptr_t)shm + wid * 2048;

  auto dma_w = [&](int wt, int slot) {
    const char* g = wbase + (size_t)wt * 64 * (HD * 2);
    dma16<0>(g, woff0, ldsw + slot * SLOT);
    dma16<0>(g, woff1, ldsw + slot * SLOT + 1024);
  };
  auto dma_kv = [&](int n) {
    const int slot = (n + 4) % NSLOT;
    const int t = n >> 1;
    if (n & 1) {
      const char* g = vbase + (size_t)t * SLOT;
      dma16<1>(g, voff0, ldsw + slot * SLOT);
      dma16<1>(g, voff1, ldsw + slot * SLOT + 1024);
    } else {
      const char* g = kbase + (size_t)t * SLOT;
      dma16<1>(g, koff0, ldsw + slot * SLOT);
      dma16<1>(g, koff1, ldsw + slot * SLOT + 1024);
    }
  };

  dma_w(0, 0); dma_w(1, 1); dma_w(2, 2); dma_w(3, 3);
  dma_kv(0); dma_kv(1); dma_kv(2); dma_kv(3); dma_kv(4);

  const int tg = (lane >> 4) & 1, tq = (lane & 15) >> 2, tp = lane & 3;
  int vra[4], wra[4];
  {
    const int tpw = ((tp & 1) << 1) | (tp >> 1);
#pragma unroll
    for (int blk = 0; blk < 4; ++blk) {
      vra[blk] = (4 * hh + tq) * 256 + 64 * (blk ^ tq) + 32 * tg + 16 * (tp >> 1) + 8 * (tp & 1);
      wra[blk] = (32 * hh + tq) * 256 + 64 * (blk ^ tq) + 32 * tg + 16 * (tpw >> 1) + 8 * (tpw & 1);
    }
  }

  f32x16 qa0 = {}, qa1 = {};
  WAIT_VM(10);
  __builtin_amdgcn_sched_barrier(0);
  BARRIER();
#define QPROJ_TILE(WT, SLOTI)                                                                    \
  {                                                                                              \
    const char* wt_ = shm + (SLOTI) * SLOT;                                                      \
    _Pragma("unroll") for (int s = 0; s < 4; ++s) {                                              \
      s16x4 a0 = tr_read(wt_ + wra[2 * kj] + s * 2048), a1 = tr_read(wt_ + wra[2 * kj] + s * 2048 + 1024); \
      s16x4 b0 = tr_read(wt_ + wra[2 * kj + 1] + s * 2048), b1 = tr_read(wt_ + wra[2 * kj + 1] + s * 2048 + 1024); \
      qa0 = MFMA32(cat44(a0, a1), xf[(WT) * 4 + s], qa0);                                        \
      qa1 = MFMA32(cat44(b0, b1), xf[(WT) * 4 + s], qa1);                                        \
    }                                                                                            \
  }
  QPROJ_TILE(0, 0) QPROJ_TILE(1, 1) QPROJ_TILE(2, 2) QPROJ_TILE(3, 3)
  BARRIER();
  dma_w(4, 0); dma_w(5, 1); dma_w(6, 2); dma_w(7, 3);
  WAIT_VM(0);
  BARRIER();
  QPROJ_TILE(4, 0) QPROJ_TILE(5, 1) QPROJ_TILE(6, 2) QPROJ_TILE(7, 3)
#undef QPROJ_TILE
  BARRIER();
  dma_kv(5); dma_kv(6);
  bf16x8 qf[8];
  {
    bf16x8 own0 = pack8(qa0[0], qa0[1], qa0[2], qa0[3], qa0[4], qa0[5], qa0[6], qa0[7]);
    bf16x8 own1 = pack8(qa0[8], qa0[9], qa0[10], qa0[11], qa0[12], qa0[13], qa0[14], qa0[15]);
    bf16x8 own2 = pack8(qa1[0], qa1[1], qa1[2], qa1[3], qa1[4], qa1[5], qa1[6], qa1[7]);
    bf16x8 own3 = pack8(qa1[8], qa1[9], qa1[10], qa1[11], qa1[12], qa1[13], qa1[14], qa1[15]);
    char* xw = shm + 2 * SLOT + wid * 4096 + lane * 16;
    *(bf16x8*)(xw) = own0;
    *(bf16x8*)(xw + 1024) = own1;
    *(bf16x8*)(xw + 2048) = own2;
    *(bf16x8*)(xw + 3072) = own3;
    BARRIER();
    const char* xr = shm + 2 * SLOT + (wid ^ 4) * 4096 + lane * 16;
    bf16x8 o0 = *(const bf16x8*)(xr), o1 = *(const bf16x8*)(xr + 1024), o2 = *(const bf16x8*)(xr + 2048), o3 = *(const bf16x8*)(xr + 3072);
    if (kj == 0) {
      qf[0] = own0; qf[1] = own1; qf[2] = own2; qf[3] = own3; qf[4] = o0; qf[5] = o1; qf[6] = o2; qf[7] = o3;
    } else {
      qf[0] = o0; qf[1] = o1; qf[2] = o2; qf[3] = o3; qf[4] = own0; qf[5] = own1; qf[6] = own2; qf[7] = own3;
    }
    BARRIER();
  }
  dma_kv(7); dma_kv(8);

  const int kra = r * 256 + ((hh ^ (r & 15)) << 4);

  constexpr float CSC = 0.088388347648318440f * 1.4426950408889634f;
  float m_run = -1e30f, l_run = 0.f;
  f32x16 o0 = {}, o1 = {}, o2 = {}, o3 = {};

  for (int s = 0; s < 16; ++s) {
    if (s == 0) { WAIT_VM(10); } else if (s < 15) { WAIT_VM(2); } else { WAIT_VM(0); }
    BARRIER();
    if (s >= 1) {
      const int n0 = 4 * s + 5;
      if (n0 < 64) dma_kv(n0);
      if (n0 + 1 < 64) dma_kv(n0 + 1);
      if (n0 + 2 < 64) dma_kv(n0 + 2);
      if (n0 + 3 < 64) dma_kv(n0 + 3);
    }
    const int t = 2 * s + kj;
    const char* kt = shm + ((2 * t + 4) % NSLOT) * SLOT;
    const char* vt = shm + ((2 * t + 5) % NSLOT) * SLOT;
    f32x16 s0 = {}, s1 = {};
#pragma unroll
    for (int c = 0; c < 8; ++c) {
      bf16x8 k0 = *(const bf16x8*)(kt + (kra ^ (c * 32)));
      bf16x8 k1 = *(const bf16x8*)(kt + (kra ^ (c * 32)) + 8192);
      s0 = MFMA32(k0, qf[c], s0);
      s1 = MFMA32(k1, qf[c], s1);
    }
    float mx = s0[0];
#pragma unroll
    for (int i = 1; i < 16; ++i) mx = fmaxf(mx, s0[i]);
#pragma unroll
    for (int i = 0; i < 16; ++i) mx = fmaxf(mx, s1[i]);
    {
      auto rr = __builtin_amdgcn_permlane32_swap(__float_as_uint(mx), __float_as_uint(mx), false, false);
      mx = fmaxf(__uint_as_float(rr[0]), __uint_as_float(rr[1]));
    }
    const float mn = fmaxf(m_run, mx);
    const float alpha = __builtin_amdgcn_exp2f((m_run - mn) * CSC);
    m_run = mn;
    const float mc = -mn * CSC;
    float ps = 0.f;
#pragma unroll
    for (int i = 0; i < 16; ++i) { s0[i] = __builtin_amdgcn_exp2f(fmaf(s0[i], CSC, mc)); ps += s0[i]; }
#pragma unroll
    for (int i = 0; i < 16; ++i) { s1[i] = __builtin_amdgcn_exp2f(fmaf(s1[i], CSC, mc)); ps += s1[i]; }
    l_run = l_run * alpha + ps;
    bf16x8 p0 = pack8(s0[0], s0[1], s0[2], s0[3], s0[4], s0[5], s0[6], s0[7]);
    bf16x8 p1 = pack8(s0[8], s0[9], s0[10], s0[11], s0[12], s0[13], s0[14], s0[15]);
    bf16x8 p2 = pack8(s1[0], s1[1], s1[2], s1[3], s1[4], s1[5], s1[6], s1[7]);
    bf16x8 p3 = pack8(s1[8], s1[9], s1[10], s1[11], s1[12], s1[13], s1[14], s1[15]);
#pragma unroll
    for (int i = 0; i < 16; ++i) { o0[i] *= alpha; o1[i] *= alpha; o2[i] *= alpha; o3[i] *= alpha; }
#define PV_BLK(OB, BLK)                                                                   \
    {                                                                                     \
      const char* vb_ = vt + vra[BLK];                                                    \
      OB = MFMA32(cat44(tr_read(vb_), tr_read(vb_ + 2048)), p0, OB);                      \
      OB = MFMA32(cat44(tr_read(vb_ + 4096), tr_read(vb_ + 6144)), p1, OB);               \
      OB = MFMA32(cat44(tr_read(vb_ + 8192), tr_read(vb_ + 10240)), p2, OB);              \
      OB = MFMA32(cat44(tr_read(vb_ + 12288), tr_read(vb_ + 14336)), p3, OB);             \
    }
    PV_BLK(o0, 0) PV_BLK(o1, 1) PV_BLK(o2, 2) PV_BLK(o3, 3)
#undef PV_BLK
  }

  {
    auto rr = __builtin_amdgcn_permlane32_swap(__float_as_uint(l_run), __float_as_uint(l_run), false, false);
    l_run = __uint_as_float(rr[0]) + __uint_as_float(rr[1]);
  }
  BARRIER();
  {
    char* mw = shm + wid * 8704;
    f32x16 sa = kj ? o0 : o2, sb = kj ? o1 : o3;
#pragma unroll
    for (int g = 0; g < 4; ++g) {
      *(f32x4*)(mw + g * 1024 + lane * 16) = (f32x4){sa[4 * g], sa[4 * g + 1], sa[4 * g + 2], sa[4 * g + 3]};
      *(f32x4*)(mw + 4096 + g * 1024 + lane * 16) = (f32x4){sb[4 * g], sb[4 * g + 1], sb[4 * g + 2], sb[4 * g + 3]};
    }
    *(float*)(mw + 8192 + lane * 4) = m_run;
    *(float*)(mw + 8448 + lane * 4) = l_run;
    BARRIER();
    const char* mr = shm + (wid ^ 4) * 8704;
    const float m2 = *(const float*)(mr + 8192 + lane * 4);
    const float l2 = *(const float*)(mr + 8448 + lane * 4);
    const float mm = fmaxf(m_run, m2);
    const float f1 = __builtin_amdgcn_exp2f((m_run - mm) * CSC), f2 = __builtin_amdgcn_exp2f((m2 - mm) * CSC);
    const float inv = 1.0f / (l_run * f1 + l2 * f2);
    const float g1 = f1 * inv, g2 = f2 * inv;
    f32x16 ka = kj ? o2 : o0, kb = kj ? o3 : o1;
    __hip_bfloat16* op = ows + (size_t)(b * SQ + qi * 32 + r) * HD + head * DH + kj * 64 + 4 * hh;
#pragma unroll
    for (int g = 0; g < 4; ++g) {
      f32x4 pa = *(const f32x4*)(mr + g * 1024 + lane * 16);
      f32x4 pb = *(const f32x4*)(mr + 4096 + g * 1024 + lane * 16);
      u32x2 wa = {cvtpk(ka[4 * g] * g1 + pa[0] * g2, ka[4 * g + 1] * g1 + pa[1] * g2),
                  cvtpk(ka[4 * g + 2] * g1 + pa[2] * g2, ka[4 * g + 3] * g1 + pa[3] * g2)};
      u32x2 wb = {cvtpk(kb[4 * g] * g1 + pb[0] * g2, kb[4 * g + 1] * g1 + pb[1] * g2),
                  cvtpk(kb[4 * g + 2] * g1 + pb[2] * g2, kb[4 * g + 3] * g1 + pb[3] * g2)};
      *(u32x2*)(op + 8 * g) = wa;
      *(u32x2*)(op + 32 + 8 * g) = wb;
    }
  }
}

constexpr int K2_NB = 4;
__global__ __launch_bounds__(256, 1) void oproj_kernel(const __hip_bfloat16* __restrict__ ows,
                                                       const __hip_bfloat16* __restrict__ wo,
                                                       __hip_bfloat16* __restrict__ out) {
  extern __shared__ __attribute__((aligned(16))) char shm[];
  const int tid = threadIdx.x;
  const int wid = __builtin_amdgcn_readfirstlane(tid >> 6);
  const int lane = tid & 63;
  const int r = lane & 31, hh = lane >> 5;
  const int b = blockIdx.x & 7, j = blockIdx.x >> 3;
  const int m0 = b * 128 + (j >> 3) * 32, n0 = (j & 7) * 64;
  const int k0 = wid * 1024;
  char* wl = shm + wid * (K2_NB * 8192);
  const int drow = lane >> 3, dslot = lane & 7;
  const int dsrc = dslot ^ (((drow >> 1) & 1) << 2);
  const char* wsrc = (const char*)(wo + (size_t)k0 * DM + n0);
  const unsigned wvoff = drow * (DM * 2) + dsrc * 16;
  const unsigned wlds = (unsigned)(uintptr_t)wl;
  auto dma_chunk = [&](int c, int buf) {
#pragma unroll
    for (int i = 0; i < 8; ++i)
      dma16<0>(wsrc + (size_t)(c * 64 + i * 8) * (DM * 2), wvoff, wlds + buf * 8192 + i * 1024);
  };
  const char* ap = (const char*)(ows + (size_t)(m0 + r) * HD + k0) + hh * 64;
  const int tg = (lane >> 4) & 1, tq = (lane & 15) >> 2, tp = lane & 3;
  int bra[2];
#pragma unroll
  for (int nb = 0; nb < 2; ++nb)
    bra[nb] = (32 * hh + tq) * 128 + (((4 * nb + 2 * tg + (tp >> 1)) ^ (((tq >> 1) & 1) << 2)) << 4) + 8 * (tp & 1);

  f32x16 c0 = {}, c1 = {};
  bf16x8 af[K2_NB][4];
#define LOADA(C, BUF)                                                                                  \
  _Pragma("unroll") for (int s = 0; s < 4; ++s)                                                        \
    asm volatile("global_load_dwordx4 %0, %1, off offset:%2" : "=v"(af[BUF][s]) : "v"(ap + (C) * 128), "i"(s * 16) : "memory");
  LOADA(0, 0) dma_chunk(0, 0);
  LOADA(1, 1) dma_chunk(1, 1);
  LOADA(2, 2) dma_chunk(2, 2);
#define K2_STEP(C, BUF, NBUF, WAITN)                                                                   \
  {                                                                                                    \
    WAIT_VM(WAITN);                                                                                    \
    __builtin_amdgcn_sched_barrier(0);                                                                 \
    const char* wb_ = wl + (BUF) * 8192;                                                               \
    _Pragma("unroll") for (int s = 0; s < 4; ++s) {                                                    \
      s16x4 a0 = tr_read(wb_ + bra[0] + s * 1024), a1 = tr_read(wb_ + bra[0] + s * 1024 + 512);        \
      s16x4 b0 = tr_read(wb_ + bra[1] + s * 1024), b1 = tr_read(wb_ + bra[1] + s * 1024 + 512);        \
      c0 = MFMA32(af[BUF][s], cat44(a0, a1), c0);                                                      \
      c1 = MFMA32(af[BUF][s], cat44(b0, b1), c1);                                                      \
    }                                                                                                  \
    asm volatile("s_waitcnt lgkmcnt(0)" ::: "memory");                                                 \
    __builtin_amdgcn_sched_barrier(0);                                                                 \
    if ((C) + 3 < 16) { LOADA((C) + 3, NBUF) dma_chunk((C) + 3, NBUF); }                               \
  }
  K2_STEP(0, 0, 3, 24) K2_STEP(1, 1, 0, 24) K2_STEP(2, 2, 1, 24) K2_STEP(3, 3, 2, 24)
  K2_STEP(4, 0, 3, 24) K2_STEP(5, 1, 0, 24) K2_STEP(6, 2, 1, 24) K2_STEP(7, 3, 2, 24)
  K2_STEP(8, 0, 3, 24) K2_STEP(9, 1, 0, 24) K2_STEP(10, 2, 1, 24) K2_STEP(11, 3, 2, 24)
  K2_STEP(12, 0, 3, 24) K2_STEP(13, 1, 0, 24) K2_STEP(14, 2, 1, 12) K2_STEP(15, 3, 2, 0)
#undef K2_STEP
#undef LOADA
  __syncthreads();
  float* red = (float*)shm;
#pragma unroll
  for (int i = 0; i < 16; ++i) {
    red[(wid * 32 + i) * 64 + lane] = c0[i];
    red[(wid * 32 + 16 + i) * 64 + lane] = c1[i];
  }
  __syncthreads();
#pragma unroll
  for (int i = 0; i < 8; ++i) {
    const int reg = wid * 8 + i;
    float v = red[reg * 64 + lane] + red[(32 + reg) * 64 + lane] + red[(64 + reg) * 64 + lane] + red[(96 + reg) * 64 + lane];
    const int rg = reg & 15;
    const int m = (rg & 3) + 8 * (rg >> 2) + 4 * hh;
    const int n = r + (reg >> 4) * 32;
    out[(size_t)(m0 + m) * DM + n0 + n] = __float2bfloat16(v);
  }
}

extern "C" void kernel_launch(void* const* d_in, const int* in_sizes, int n_in,
                              void* d_out, int out_size, void* d_ws, size_t ws_size,
                              hipStream_t stream) {
  const __hip_bfloat16* x = (const __hip_bfloat16*)d_in[0];
  const __hip_bfloat16* kc = (const __hip_bfloat16*)d_in[1];
  const __hip_bfloat16* vc = (const __hip_bfloat16*)d_in[2];
  const __hip_bfloat16* wq = (const __hip_bfloat16*)d_in[3];
  const __hip_bfloat16* wo = (const __hip_bfloat16*)d_in[4];
  __hip_bfloat16* out = (__hip_bfloat16*)d_out;
  __hip_bfloat16* ows = (__hip_bfloat16*)d_ws;
  hipFuncSetAttribute((const void*)attn_kernel, hipFuncAttributeMaxDynamicSharedMemorySize, LDS1);
  hipFuncSetAttribute((const void*)oproj_kernel, hipFuncAttributeMaxDynamicSharedMemorySize, 4 * K2_NB * 8192);
  attn_kernel<<<256, 512, LDS1, stream>>>(x, kc, vc, wq, ows);
  oproj_kernel<<<256, 256, 4 * K2_NB * 8192, stream>>>(ows, wo, out);
}
```

```cpp
#include <hip/hip_runtime.h>
#include <hip/hip_bf16.h>
#include <stdint.h>

typedef __attribute__((ext_vector_type(8))) short bf16x8;
typedef __attribute__((ext_vector_type(4))) short s16x4;
typedef __attribute__((ext_vector_type(16))) float f32x16;
typedef __attribute__((ext_vector_type(4))) float f32x4;
typedef __attribute__((ext_vector_type(4))) unsigned u32x4;
typedef __attribute__((ext_vector_type(2))) unsigned u32x2;
typedef __attribute__((address_space(3))) s16x4* lds_s16x4_ptr;

constexpr int Bn = 8, SQ = 128, DM = 512, NH = 32, SK = 2048, DH = 128;
constexpr int HD = NH * DH;
constexpr int SLOT = 16384;
constexpr int NSLOT = 10;
constexpr int LDS1 = NSLOT * SLOT;

typedef __attribute__((ext_vector_type(2))) float f32x2;
typedef __attribute__((ext_vector_type(2))) __bf16 bf16x2_t;
__device__ __forceinline__ unsigned cvtpk(float lo, float hi) {
  f32x2 v = {lo, hi};
  bf16x2_t r = __builtin_convertvector(v, bf16x2_t);
  return __builtin_bit_cast(unsigned, r);
}
__device__ __forceinline__ bf16x8 pack8(float a0, float a1, float a2, float a3, float a4, float a5, float a6, float a7) {
  u32x4 w = {cvtpk(a0, a1), cvtpk(a2, a3), cvtpk(a4, a5), cvtpk(a6, a7)};
  return __builtin_bit_cast(bf16x8, w);
}
__device__ __forceinline__ s16x4 tr_read(const char* p) {
  return __builtin_amdgcn_ds_read_tr16_b64_v4i16((lds_s16x4_ptr)(p));
}
__device__ __forceinline__ bf16x8 cat44(s16x4 a, s16x4 b) {
  return (bf16x8){a[0], a[1], a[2], a[3], b[0], b[1], b[2], b[3]};
}
#define MFMA32(a, b, c) __builtin_amdgcn_mfma_f32_32x32x16_bf16((a), (b), (c), 0, 0, 0)
#define WAIT_VM(n) asm volatile("s_waitcnt vmcnt(" #n ")" ::: "memory")
#define BARRIER() do { asm volatile("s_waitcnt lgkmcnt(0)" ::: "memory"); __builtin_amdgcn_s_barrier(); asm volatile("" ::: "memory"); } while (0)

#define TS(slot)
#define TSW(slot)
template <int NT>
__device__ __forceinline__ void dma16(const void* sbase, unsigned voff, unsigned lds) {
  if constexpr (NT)
    asm volatile("s_mov_b32 m0, %2\n\ts_nop 0\n\tglobal_load_lds_dwordx4 %0, %1 nt" :: "v"(voff), "s"(sbase), "s"(lds) : "memory");
  else
    asm volatile("s_mov_b32 m0, %2\n\ts_nop 0\n\tglobal_load_lds_dwordx4 %0, %1" :: "v"(voff), "s"(sbase), "s"(lds) : "memory");
}

__global__ __launch_bounds__(512, 2) void attn_kernel(const __hip_bfloat16* __restrict__ x,
                                                      const __hip_bfloat16* __restrict__ kc,
                                                      const __hip_bfloat16* __restrict__ vc,
                                                      const __hip_bfloat16* __restrict__ wq,
                                                      const __hip_bfloat16* __restrict__ wo,
                                                      __hip_bfloat16* __restrict__ ows) {
  extern __shared__ __attribute__((aligned(16))) char shm[];
  const int tid = threadIdx.x;
  const int wid = __builtin_amdgcn_readfirstlane(tid >> 6);
  const int lane = tid & 63;
  const int r = lane & 31, hh = lane >> 5;
  const int qi = wid & 3, kj = wid >> 2;
  const int b = blockIdx.x >> 5, head = blockIdx.x & 31;

  const char* kbase = (const char*)(kc + (size_t)(b * NH + head) * SK * DH);
  const char* vbase = (const char*)(vc + (size_t)(b * NH + head) * SK * DH);
  const unsigned lds0 = (unsigned)(uintptr_t)shm;
  TS(51);

  const int drow = lane >> 4, dslot = lane & 15;
  const int krow0 = wid * 8 + drow, krow1 = krow0 + 4;
  const unsigned koff0 = krow0 * 256 + ((dslot ^ (krow0 & 15)) << 4);
  const unsigned koff1 = krow1 * 256 + ((dslot ^ (krow1 & 15)) << 4);
  const unsigned voff0 = krow0 * 256 + ((dslot ^ (drow << 2)) << 4);
  const unsigned voff1 = voff0 + 1024;
  const unsigned woff0 = krow0 * (HD * 2) + ((dslot ^ (drow << 2)) << 4);
  const unsigned woff1 = woff0 + 4 * (HD * 2);
  const int xrow0 = wid * 16 + (lane >> 3), xrow1 = xrow0 + 8;
  const unsigned xoff0 = xrow0 * (DM * 2) + (((lane & 7) ^ ((xrow0 >> 1) & 7)) << 4);
  const unsigned xoff1 = xrow1 * (DM * 2) + (((lane & 7) ^ ((xrow1 >> 1) & 7)) << 4);
  const unsigned ldsw = lds0 + wid * 2048;
  const char* wbase = (const char*)(wq + head * DH);
  const char* xbase = (const char*)(x + (size_t)b * SQ * DM);
  auto dma_k = [&](int t, int slot) {
    const char* g = kbase + (size_t)t * SLOT;
    dma16<1>(g, koff0, ldsw + slot * SLOT);
    dma16<1>(g, koff1, ldsw + slot * SLOT + 1024);
  };
  auto dma_v = [&](int t, int slot) {
    const char* g = vbase + (size_t)t * SLOT;
    dma16<1>(g, voff0, ldsw + slot * SLOT);
    dma16<1>(g, voff1, ldsw + slot * SLOT + 1024);
  };
  auto dma_wx = [&](int w) {
    const int sw = (2 * w) % NSLOT;
    const int wr = (w + head) & 7;
    const char* g = wbase + (size_t)wr * 64 * (HD * 2);
    dma16<0>(g, woff0, ldsw + sw * SLOT);
    dma16<0>(g, woff1, ldsw + sw * SLOT + 1024);
    const char* gx = xbase + wr * 128;
    dma16<0>(gx, xoff0, ldsw + (sw + 1) * SLOT);
    dma16<0>(gx, xoff1, ldsw + (sw + 1) * SLOT + 1024);
  };
  dma_wx(0); dma_wx(1); dma_wx(2); dma_wx(3); dma_wx(4);

  const int tg = (lane >> 4) & 1, tq = (lane & 15) >> 2, tp = lane & 3;
  int vra[4], wra[2];
  {
    const int tpw = ((tp & 1) << 1) | (tp >> 1);
#pragma unroll
    for (int blk = 0; blk < 4; ++blk)
      vra[blk] = (4 * hh + tq) * 256 + 64 * (blk ^ tq) + 32 * tg + 16 * (tp >> 1) + 8 * (tp & 1);
#pragma unroll
    for (int u = 0; u < 2; ++u)
      wra[u] = (32 * hh + tq) * 256 + 64 * ((2 * kj + u) ^ tq) + 32 * tg + 16 * (tpw >> 1) + 8 * (tpw & 1);
  }
  const int xra = (qi * 32 + r) * 128 + (((4 * hh) ^ ((r >> 1) & 7)) << 4);

  f32x16 qa0 = {}, qa1 = {};
#pragma unroll
  for (int w = 0; w < 8; ++w) {
    if (w == 0) { WAIT_VM(16); } else { WAIT_VM(12); }
    BARRIER();
    TS(w);
    if (w >= 1 && w <= 3) dma_wx(w + 4);
    if (w == 4) { dma_k(0, 6); dma_k(1, 7); }
    if (w == 5) { dma_v(0, 8); dma_k(2, 9); }
    if (w == 6) { dma_v(1, 0); dma_k(3, 1); }
    if (w == 7) { dma_v(2, 2); dma_k(4, 3); }
    const char* wt_ = shm + ((2 * w) % NSLOT) * SLOT;
    const char* xt_ = wt_ + SLOT;
#pragma unroll
    for (int s = 0; s < 4; ++s) {
      bf16x8 xf = *(const bf16x8*)(xt_ + (xra ^ (s << 4)));
      s16x4 a0 = tr_read(wt_ + wra[0] + s * 2048), a1 = tr_read(wt_ + wra[0] + s * 2048 + 1024);
      s16x4 b0 = tr_read(wt_ + wra[1] + s * 2048), b1 = tr_read(wt_ + wra[1] + s * 2048 + 1024);
      qa0 = MFMA32(cat44(a0, a1), xf, qa0);
      qa1 = MFMA32(cat44(b0, b1), xf, qa1);
    }
  }
  BARRIER();
  bf16x8 qf[8];
  {
    bf16x8 own0 = pack8(qa0[0], qa0[1], qa0[2], qa0[3], qa0[4], qa0[5], qa0[6], qa0[7]);
    bf16x8 own1 = pack8(qa0[8], qa0[9], qa0[10], qa0[11], qa0[12], qa0[13], qa0[14], qa0[15]);
    bf16x8 own2 = pack8(qa1[0], qa1[1], qa1[2], qa1[3], qa1[4], qa1[5], qa1[6], qa1[7]);
    bf16x8 own3 = pack8(qa1[8], qa1[9], qa1[10], qa1[11], qa1[12], qa1[13], qa1[14], qa1[15]);
    char* xw = shm + 4 * SLOT + wid * 4096 + lane * 16;
    *(bf16x8*)(xw) = own0;
    *(bf16x8*)(xw + 1024) = own1;
    *(bf16x8*)(xw + 2048) = own2;
    *(bf16x8*)(xw + 3072) = own3;
    BARRIER();
    const char* xr = shm + 4 * SLOT + (wid ^ 4) * 4096 + lane * 16;
    bf16x8 o0 = *(const bf16x8*)(xr), o1 = *(const bf16x8*)(xr + 1024), o2 = *(const bf16x8*)(xr + 2048), o3 = *(const bf16x8*)(xr + 3072);
    if (kj == 0) {
      qf[0] = own0; qf[1] = own1; qf[2] = own2; qf[3] = own3; qf[4] = o0; qf[5] = o1; qf[6] = o2; qf[7] = o3;
    } else {
      qf[0] = o0; qf[1] = o1; qf[2] = o2; qf[3] = o3; qf[4] = own0; qf[5] = own1; qf[6] = own2; qf[7] = own3;
    }
  }

  auto sync_iv = [&](int i) {
    if (i < 30) { WAIT_VM(12); } else if (i == 30) { WAIT_VM(10); } else if (i == 31) { WAIT_VM(6); } else if (i == 32) { WAIT_VM(4); } else { WAIT_VM(2); }
    BARRIER();
    TS(8 + i);
    if (i >= 3) {
      if (i <= 28) {
        const int j0 = 2 * i + 6;
        dma_v(i + 2, (j0 + 6) % NSLOT);
        if (i < 28) dma_k(i + 4, (j0 + 7) % NSLOT); else dma_v(31, (j0 + 7) % NSLOT);
      } else if (i == 29) {
        const char* g = (const char*)wo + (size_t)blockIdx.x * 16384 + wid * 2048;
        dma16<0>(g, lane * 16, ldsw);
        dma16<0>(g + 1024, lane * 16, ldsw + 1024);
      }
    } else if (i == 0) {
      dma_v(3, 4); dma_k(5, 5);
    } else if (i == 1) {
      dma_v(4, 6);
    } else {
      dma_k(6, 7);
    }
  };

  const int kra = r * 256 + ((hh ^ (r & 15)) << 4);

  constexpr float CSC = 0.088388347648318440f * 1.4426950408889634f;
  float m_run = -1e30f, l_run = 0.f;
  f32x16 o0 = {}, o1 = {}, o2 = {}, o3 = {};
  bf16x8 p0 = {}, p1 = {}, p2 = {}, p3 = {};

#define PV_BLK(OB, BLK)                                                                   \
    {                                                                                     \
      const char* vb_ = vt + vra[BLK];                                                    \
      OB = MFMA32(cat44(tr_read(vb_), tr_read(vb_ + 2048)), p0, OB);                      \
      OB = MFMA32(cat44(tr_read(vb_ + 4096), tr_read(vb_ + 6144)), p1, OB);               \
      OB = MFMA32(cat44(tr_read(vb_ + 8192), tr_read(vb_ + 10240)), p2, OB);              \
      OB = MFMA32(cat44(tr_read(vb_ + 12288), tr_read(vb_ + 14336)), p3, OB);             \
    }

  if (kj == 1) sync_iv(0);
  for (int tt = 0; tt < 16; ++tt) {
    const int t = 2 * tt + kj;
    sync_iv(t);
    if (tt > 0) {
      const char* vt = shm + ((2 * t + 4) % NSLOT) * SLOT;
      PV_BLK(o0, 0) PV_BLK(o1, 1) PV_BLK(o2, 2) PV_BLK(o3, 3)
    }
    const char* kt = shm + (((t < 2 ? t : 2 * t - 1) + 6) % NSLOT) * SLOT;
    f32x16 s0 = {}, s1 = {};
#pragma unroll
    for (int c = 0; c < 8; ++c) {
      bf16x8 k0 = *(const bf16x8*)(kt + (kra ^ (c * 32)));
      bf16x8 k1 = *(const bf16x8*)(kt + (kra ^ (c * 32)) + 8192);
      s0 = MFMA32(k0, qf[c], s0);
      s1 = MFMA32(k1, qf[c], s1);
    }
    sync_iv(t + 1);
    float mx = s0[0];
#pragma unroll
    for (int i = 1; i < 16; ++i) mx = fmaxf(mx, s0[i]);
#pragma unroll
    for (int i = 0; i < 16; ++i) mx = fmaxf(mx, s1[i]);
    {
      auto rr = __builtin_amdgcn_permlane32_swap(__float_as_uint(mx), __float_as_uint(mx), false, false);
      mx = fmaxf(__uint_as_float(rr[0]), __uint_as_float(rr[1]));
    }
    const float mn = fmaxf(m_run, mx);
    const float alpha = __builtin_amdgcn_exp2f((m_run - mn) * CSC);
    m_run = mn;
    const float mc = -mn * CSC;
    float ps = 0.f;
#pragma unroll
    for (int i = 0; i < 16; ++i) { s0[i] = __builtin_amdgcn_exp2f(fmaf(s0[i], CSC, mc)); ps += s0[i]; }
#pragma unroll
    for (int i = 0; i < 16; ++i) { s1[i] = __builtin_amdgcn_exp2f(fmaf(s1[i], CSC, mc)); ps += s1[i]; }
    l_run = l_run * alpha + ps;
    p0 = pack8(s0[0], s0[1], s0[2], s0[3], s0[4], s0[5], s0[6], s0[7]);
    p1 = pack8(s0[8], s0[9], s0[10], s0[11], s0[12], s0[13], s0[14], s0[15]);
    p2 = pack8(s1[0], s1[1], s1[2], s1[3], s1[4], s1[5], s1[6], s1[7]);
    p3 = pack8(s1[8], s1[9], s1[10], s1[11], s1[12], s1[13], s1[14], s1[15]);
#pragma unroll
    for (int i = 0; i < 16; ++i) { o0[i] *= alpha; o1[i] *= alpha; o2[i] *= alpha; o3[i] *= alpha; }
  }
  {
    sync_iv(32 + kj);
    const char* vt = shm + ((68 + kj) % NSLOT) * SLOT;
    PV_BLK(o0, 0) PV_BLK(o1, 1) PV_BLK(o2, 2) PV_BLK(o3, 3)
    if (kj == 0) sync_iv(33);
  }
#undef PV_BLK

  {
    auto rr = __builtin_amdgcn_permlane32_swap(__float_as_uint(l_run), __float_as_uint(l_run), false, false);
    l_run = __uint_as_float(rr[0]) + __uint_as_float(rr[1]);
  }
  {
    char* mw = shm + 2 * SLOT + wid * 8704;
    f32x16 sa = kj ? o0 : o2, sb = kj ? o1 : o3;
#pragma unroll
    for (int g = 0; g < 4; ++g) {
      *(f32x4*)(mw + g * 1024 + lane * 16) = (f32x4){sa[4 * g], sa[4 * g + 1], sa[4 * g + 2], sa[4 * g + 3]};
      *(f32x4*)(mw + 4096 + g * 1024 + lane * 16) = (f32x4){sb[4 * g], sb[4 * g + 1], sb[4 * g + 2], sb[4 * g + 3]};
    }
    *(float*)(mw + 8192 + lane * 4) = m_run;
    *(float*)(mw + 8448 + lane * 4) = l_run;
    WAIT_VM(0);
    BARRIER();
    {
      const char* wp = shm + (4 * hh + tq) * 1024 + (wid * 64 + 16 * tg + 4 * tp) * 2;
      bf16x8 f0 = cat44(tr_read(wp), tr_read(wp + 8192));
      bf16x8 f1 = cat44(tr_read(wp + 64), tr_read(wp + 8192 + 64));
      char* bp = (char*)ows + (8u << 20) + ((size_t)(wid * 256 + blockIdx.x) << 11) + lane * 16;
      *(bf16x8*)(bp) = f0;
      *(bf16x8*)(bp + 1024) = f1;
    }
    const char* mr = shm + 2 * SLOT + (wid ^ 4) * 8704;
    const float m2 = *(const float*)(mr + 8192 + lane * 4);
    const float l2 = *(const float*)(mr + 8448 + lane * 4);
    const float mm = fmaxf(m_run, m2);
    const float f1 = __builtin_amdgcn_exp2f((m_run - mm) * CSC), f2 = __builtin_amdgcn_exp2f((m2 - mm) * CSC);
    const float inv = 1.0f / (l_run * f1 + l2 * f2);
    const float g1 = f1 * inv, g2 = f2 * inv;
    f32x16 ka = kj ? o2 : o0, kb = kj ? o3 : o1;
    char* op = (char*)ows + ((size_t)((b * 4 + qi) * 256 + head * 8 + kj * 4) << 10) + lane * 16;
#pragma unroll
    for (int gp = 0; gp < 2; ++gp) {
      f32x4 pa0 = *(const f32x4*)(mr + (2 * gp) * 1024 + lane * 16), pa1 = *(const f32x4*)(mr + (2 * gp + 1) * 1024 + lane * 16);
      f32x4 pb0 = *(const f32x4*)(mr + 4096 + (2 * gp) * 1024 + lane * 16), pb1 = *(const f32x4*)(mr + 4096 + (2 * gp + 1) * 1024 + lane * 16);
      u32x4 wa = {cvtpk(ka[8 * gp] * g1 + pa0[0] * g2, ka[8 * gp + 1] * g1 + pa0[1] * g2),
                  cvtpk(ka[8 * gp + 2] * g1 + pa0[2] * g2, ka[8 * gp + 3] * g1 + pa0[3] * g2),
                  cvtpk(ka[8 * gp + 4] * g1 + pa1[0] * g2, ka[8 * gp + 5] * g1 + pa1[1] * g2),
                  cvtpk(ka[8 * gp + 6] * g1 + pa1[2] * g2, ka[8 * gp + 7] * g1 + pa1[3] * g2)};
      u32x4 wb = {cvtpk(kb[8 * gp] * g1 + pb0[0] * g2, kb[8 * gp + 1] * g1 + pb0[1] * g2),
                  cvtpk(kb[8 * gp + 2] * g1 + pb0[2] * g2, kb[8 * gp + 3] * g1 + pb0[3] * g2),
                  cvtpk(kb[8 * gp + 4] * g1 + pb1[0] * g2, kb[8 * gp + 5] * g1 + pb1[1] * g2),
                  cvtpk(kb[8 * gp + 6] * g1 + pb1[2] * g2, kb[8 * gp + 7] * g1 + pb1[3] * g2)};
      *(u32x4*)(op + gp * 1024) = wa;
      *(u32x4*)(op + 2048 + gp * 1024) = wb;
    }
    TS(50);
  }
}

constexpr int K2_LDS = 65536;
constexpr int K2_D = 8;
__global__ __launch_bounds__(512, 2) void oproj_kernel(const __hip_bfloat16* __restrict__ ows,
                                                       __hip_bfloat16* __restrict__ out) {
  extern __shared__ __attribute__((aligned(16))) char shm[];
  const int tid = threadIdx.x;
  const int wid = __builtin_amdgcn_readfirstlane(tid >> 6);
  const int lane = tid & 63;
  const int r = lane & 31, hh = lane >> 5;
  const int b = blockIdx.x & 7, j = blockIdx.x >> 3;
  const int mq = j >> 3, nq = j & 7;
  const int m0 = b * 128 + mq * 32, n0 = nq * 64;
  TS(0);
  const char* ap = (const char*)ows + ((size_t)((b * 4 + mq) * 256 + wid * 32) << 10) + lane * 16;
  const char* bp = (const char*)ows + (8u << 20) + ((size_t)(nq * 256 + wid * 32) << 11) + lane * 16;
  f32x16 c0 = {}, c1 = {};
  bf16x8 af[K2_D], b0f[K2_D], b1f[K2_D];
#pragma unroll
  for (int i = 0; i < K2_D; ++i) {
    asm volatile("global_load_dwordx4 %0, %1, off" : "=v"(af[i]) : "v"(ap + i * 1024) : "memory");
    asm volatile("global_load_dwordx4 %0, %1, off" : "=v"(b0f[i]) : "v"(bp + i * 2048) : "memory");
    asm volatile("global_load_dwordx4 %0, %1, off offset:1024" : "=v"(b1f[i]) : "v"(bp + i * 2048) : "memory");
  }
#pragma unroll
  for (int ks = 0; ks < 32; ++ks) {
    const int i = ks % K2_D;
    if (ks <= 32 - K2_D) { WAIT_VM(21); }
    else if (ks == 25) { WAIT_VM(18); } else if (ks == 26) { WAIT_VM(15); } else if (ks == 27) { WAIT_VM(12); }
    else if (ks == 28) { WAIT_VM(9); } else if (ks == 29) { WAIT_VM(6); } else if (ks == 30) { WAIT_VM(3); } else { WAIT_VM(0); }
    __builtin_amdgcn_sched_barrier(0);
    c0 = MFMA32(af[i], b0f[i], c0);
    c1 = MFMA32(af[i], b1f[i], c1);
    __builtin_amdgcn_sched_barrier(0);
    if (ks + K2_D < 32) {
      asm volatile("global_load_dwordx4 %0, %1, off" : "=v"(af[i]) : "v"(ap + (ks + K2_D) * 1024) : "memory");
      asm volatile("global_load_dwordx4 %0, %1, off" : "=v"(b0f[i]) : "v"(bp + (ks + K2_D) * 2048) : "memory");
      asm volatile("global_load_dwordx4 %0, %1, off offset:1024" : "=v"(b1f[i]) : "v"(bp + (ks + K2_D) * 2048) : "memory");
    }
  }
  TSW(24);
  float* red = (float*)shm;
#pragma unroll
  for (int i = 0; i < 16; ++i) {
    red[(wid * 32 + i) * 64 + lane] = c0[i];
    red[(wid * 32 + 16 + i) * 64 + lane] = c1[i];
  }
  __syncthreads();
#pragma unroll
  for (int i = 0; i < 4; ++i) {
    const int reg = wid * 4 + i;
    float v = 0.f;
#pragma unroll
    for (int w = 0; w < 8; ++w) v += red[(w * 32 + reg) * 64 + lane];
    const int rg = reg & 15;
    const int m = (rg & 3) + 8 * (rg >> 2) + 4 * hh;
    const int n = r + (reg >> 4) * 32;
    out[(size_t)(m0 + m) * DM + n0 + n] = __float2bfloat16(v);
  }
  TSW(40);
}

extern "C" void kernel_launch(void* const* d_in, const int* in_sizes, int n_in,
                              void* d_out, int out_size, void* d_ws, size_t ws_size,
                              hipStream_t stream) {
  const __hip_bfloat16* x = (const __hip_bfloat16*)d_in[0];
  const __hip_bfloat16* kc = (const __hip_bfloat16*)d_in[1];
  const __hip_bfloat16* vc = (const __hip_bfloat16*)d_in[2];
  const __hip_bfloat16* wq = (const __hip_bfloat16*)d_in[3];
  const __hip_bfloat16* wo = (const __hip_bfloat16*)d_in[4];
  __hip_bfloat16* out = (__hip_bfloat16*)d_out;
  __hip_bfloat16* ows = (__hip_bfloat16*)d_ws;
  hipFuncSetAttribute((const void*)attn_kernel, hipFuncAttributeMaxDynamicSharedMemorySize, LDS1);
  hipFuncSetAttribute((const void*)oproj_kernel, hipFuncAttributeMaxDynamicSharedMemorySize, K2_LDS);
  attn_kernel<<<256, 512, LDS1, stream>>>(x, kc, vc, wq, wo, ows);
  oproj_kernel<<<256, 512, K2_LDS, stream>>>(ows, out);
}
```

```cpp
#include <hip/hip_runtime.h>
#include <hip/hip_bf16.h>
#include <stdint.h>
#include <type_traits>

typedef __attribute__((ext_vector_type(8))) short bf16x8;
typedef __attribute__((ext_vector_type(4))) short s16x4;
typedef __attribute__((ext_vector_type(16))) float f32x16;
typedef __attribute__((ext_vector_type(4))) float f32x4;
typedef __attribute__((ext_vector_type(4))) unsigned u32x4;
typedef __attribute__((ext_vector_type(2))) unsigned u32x2;
typedef __attribute__((address_space(3))) s16x4* lds_s16x4_ptr;

constexpr int Bn = 8, SQ = 128, DM = 512, NH = 32, SK = 2048, DH = 128;
constexpr int HD = NH * DH;
constexpr int SLOT = 16384;
constexpr int NSLOT = 10;
constexpr int LDS1 = NSLOT * SLOT;

typedef __attribute__((ext_vector_type(2))) float f32x2;
typedef __attribute__((ext_vector_type(2))) __bf16 bf16x2_t;
__device__ __forceinline__ unsigned cvtpk(float lo, float hi) {
  f32x2 v = {lo, hi};
  bf16x2_t r = __builtin_convertvector(v, bf16x2_t);
  return __builtin_bit_cast(unsigned, r);
}
__device__ __forceinline__ bf16x8 pack8(float a0, float a1, float a2, float a3, float a4, float a5, float a6, float a7) {
  u32x4 w = {cvtpk(a0, a1), cvtpk(a2, a3), cvtpk(a4, a5), cvtpk(a6, a7)};
  return __builtin_bit_cast(bf16x8, w);
}
__device__ __forceinline__ s16x4 tr_read(const char* p) {
  return __builtin_amdgcn_ds_read_tr16_b64_v4i16((lds_s16x4_ptr)(p));
}
__device__ __forceinline__ bf16x8 cat44(s16x4 a, s16x4 b) {
  return (bf16x8){a[0], a[1], a[2], a[3], b[0], b[1], b[2], b[3]};
}
#define MFMA32(a, b, c) __builtin_amdgcn_mfma_f32_32x32x16_bf16((a), (b), (c), 0, 0, 0)
#define WAIT_VM(n) asm volatile("s_waitcnt vmcnt(" #n ")" ::: "memory")
#define BARRIER() do { asm volatile("s_waitcnt lgkmcnt(0)" ::: "memory"); __builtin_amdgcn_s_barrier(); asm volatile("" ::: "memory"); } while (0)

#define TS(slot)
#define TSW(slot)
#define TSR(slot)
template <int NT>
__device__ __forceinline__ void dma16(const void* sbase, unsigned voff, unsigned lds) {
  if constexpr (NT)
    asm volatile("s_mov_b32 m0, %2\n\ts_nop 0\n\tglobal_load_lds_dwordx4 %0, %1 nt" :: "v"(voff), "s"(sbase), "s"(lds) : "memory");
  else
    asm volatile("s_mov_b32 m0, %2\n\ts_nop 0\n\tglobal_load_lds_dwordx4 %0, %1" :: "v"(voff), "s"(sbase), "s"(lds) : "memory");
}

__global__ __launch_bounds__(512, 2) void attn_kernel(const __hip_bfloat16* __restrict__ x,
                                                      const __hip_bfloat16* __restrict__ kc,
                                                      const __hip_bfloat16* __restrict__ vc,
                                                      const __hip_bfloat16* __restrict__ wq,
                                                      const __hip_bfloat16* __restrict__ wo,
                                                      __hip_bfloat16* __restrict__ ows) {
  extern __shared__ __attribute__((aligned(16))) char shm[];
  const int tid = threadIdx.x;
  const int wid = __builtin_amdgcn_readfirstlane(tid >> 6);
  const int lane = tid & 63;
  const int r = lane & 31, hh = lane >> 5;
  const int qi = wid & 3, kj = wid >> 2;
  const int b = blockIdx.x >> 5, head = blockIdx.x & 31;

  const char* kbase = (const char*)(kc + (size_t)(b * NH + head) * SK * DH);
  const char* vbase = (const char*)(vc + (size_t)(b * NH + head) * SK * DH);
  const unsigned lds0 = (unsigned)(uintptr_t)shm;
  TS(51); TSR(52);

  const int drow = lane >> 4, dslot = lane & 15;
  const int krow0 = wid * 8 + drow, krow1 = krow0 + 4;
  const unsigned koff0 = krow0 * 256 + ((dslot ^ (krow0 & 15)) << 4);
  const unsigned koff1 = krow1 * 256 + ((dslot ^ (krow1 & 15)) << 4);
  const unsigned voff0 = krow0 * 256 + ((dslot ^ (drow << 2)) << 4);
  const unsigned voff1 = voff0 + 1024;
  const unsigned woff0 = krow0 * (HD * 2) + ((dslot ^ (drow << 2)) << 4);
  const unsigned woff1 = woff0 + 4 * (HD * 2);
  const int xrow0 = wid * 16 + (lane >> 3), xrow1 = xrow0 + 8;
  const unsigned xoff0 = xrow0 * (DM * 2) + (((lane & 7) ^ ((xrow0 >> 1) & 7)) << 4);
  const unsigned xoff1 = xrow1 * (DM * 2) + (((lane & 7) ^ ((xrow1 >> 1) & 7)) << 4);
  const unsigned ldsw = lds0 + wid * 2048;
  const char* wbase = (const char*)(wq + head * DH);
  const char* xbase = (const char*)(x + (size_t)b * SQ * DM);
#define WNT 0
#define XNT 0
#define OMAP 0
#define QPIPE 1
#define TMASK 31
#define ROTM 0
#define ROTV 0
  const int rot = (blockIdx.x * ROTM + (blockIdx.x >> 5) * ROTV) & 31;
  auto dma_k = [&](int t, int slot) {
    const char* g = kbase + (size_t)((t + rot) & TMASK) * SLOT;
    dma16<1>(g, koff0, ldsw + slot * SLOT);
    dma16<1>(g, koff1, ldsw + slot * SLOT + 1024);
  };
  auto dma_v = [&](int t, int slot) {
    const char* g = vbase + (size_t)((t + rot) & TMASK) * SLOT;
    dma16<1>(g, voff0, ldsw + slot * SLOT);
    dma16<1>(g, voff1, ldsw + slot * SLOT + 1024);
  };
  auto dma_wx = [&](int w) {
    const int sw = (2 * w) % NSLOT;
    const int wr = (w + head) & 7;
    const char* g = wbase + (size_t)wr * 64 * (HD * 2);
    dma16<WNT>(g, woff0, ldsw + sw * SLOT);
    dma16<WNT>(g, woff1, ldsw + sw * SLOT + 1024);
    const char* gx = xbase + wr * 128;
    dma16<XNT>(gx, xoff0, ldsw + (sw + 1) * SLOT);
    dma16<XNT>(gx, xoff1, ldsw + (sw + 1) * SLOT + 1024);
  };
  dma_wx(0); dma_wx(1); dma_wx(2); dma_wx(3); dma_wx(4);
#define PFN 0
#define PFPOL ""
#define PFOFF 0
#define PFSTR 128
  u32x4 pfd = {0u, 0u, 0u, 0u};
  {
    const char* pb = (kj ? vbase : kbase) + PFOFF * SLOT + qi * (PFN * 1024);
#pragma unroll
    for (int i = 0; i < PFN; ++i)
      asm volatile("global_load_dwordx4 %0, %1, %2 " PFPOL : "+v"(pfd) : "v"(lane * 16 + i * 1024), "s"(pb) : "memory");
  }

  const int tg = (lane >> 4) & 1, tq = (lane & 15) >> 2, tp = lane & 3;
  int vra[4], wra[2];
  {
    const int tpw = ((tp & 1) << 1) | (tp >> 1);
#pragma unroll
    for (int blk = 0; blk < 4; ++blk)
      vra[blk] = (4 * hh + tq) * 256 + 64 * (blk ^ tq) + 32 * tg + 16 * (tp >> 1) + 8 * (tp & 1);
#pragma unroll
    for (int u = 0; u < 2; ++u)
      wra[u] = (32 * hh + tq) * 256 + 64 * ((2 * kj + u) ^ tq) + 32 * tg + 16 * (tpw >> 1) + 8 * (tpw & 1);
  }
  const int xra = (qi * 32 + r) * 128 + (((4 * hh) ^ ((r >> 1) & 7)) << 4);

  f32x16 qa0 = {}, qa1 = {};
#pragma unroll
  for (int w = 0; w < 8; ++w) {
    if (w == 0) { asm volatile("s_waitcnt vmcnt(%0)" :: "n"(16 + PFN) : "memory"); }
    else if (w <= 4) { asm volatile("s_waitcnt vmcnt(%0)" :: "n"(12 + PFN) : "memory"); }
    else { WAIT_VM(12); }
    if (w == 5) {
      if (PFN > 0) asm volatile("" :: "v"(pfd));
    }
    BARRIER();
    TS(w);
    if (w >= 1 && w <= 3) dma_wx(w + 4);
    if (w == 4) { dma_k(0, 6); dma_k(1, 7); }
    if (w == 5) { dma_v(0, 8); dma_k(2, 9); }
    if (w == 6) { dma_v(1, 0); dma_k(3, 1); }
    if (w == 7) { dma_v(2, 2); dma_k(4, 3); }
    const char* wt_ = shm + ((2 * w) % NSLOT) * SLOT;
    const char* xt_ = wt_ + SLOT;
    {
      bf16x8 xf[4], wa[4], wb[4];
#pragma unroll
      for (int s = 0; s < 4; ++s) {
        xf[s] = *(const bf16x8*)(xt_ + (xra ^ (s << 4)));
        wa[s] = cat44(tr_read(wt_ + wra[0] + s * 2048), tr_read(wt_ + wra[0] + s * 2048 + 1024));
        wb[s] = cat44(tr_read(wt_ + wra[1] + s * 2048), tr_read(wt_ + wra[1] + s * 2048 + 1024));
      }
      __builtin_amdgcn_sched_barrier(0);
#pragma unroll
      for (int s = 0; s < 4; ++s) {
        qa0 = MFMA32(wa[s], xf[s], qa0);
        qa1 = MFMA32(wb[s], xf[s], qa1);
      }
      __builtin_amdgcn_sched_barrier(0);
    }
  }
  BARRIER();
  bf16x8 qf[8];
  {
    bf16x8 own0 = pack8(qa0[0], qa0[1], qa0[2], qa0[3], qa0[4], qa0[5], qa0[6], qa0[7]);
    bf16x8 own1 = pack8(qa0[8], qa0[9], qa0[10], qa0[11], qa0[12], qa0[13], qa0[14], qa0[15]);
    bf16x8 own2 = pack8(qa1[0], qa1[1], qa1[2], qa1[3], qa1[4], qa1[5], qa1[6], qa1[7]);
    bf16x8 own3 = pack8(qa1[8], qa1[9], qa1[10], qa1[11], qa1[12], qa1[13], qa1[14], qa1[15]);
    char* xw = shm + 4 * SLOT + wid * 4096 + lane * 16;
    *(bf16x8*)(xw) = own0;
    *(bf16x8*)(xw + 1024) = own1;
    *(bf16x8*)(xw + 2048) = own2;
    *(bf16x8*)(xw + 3072) = own3;
    BARRIER();
    const char* xr = shm + 4 * SLOT + (wid ^ 4) * 4096 + lane * 16;
    bf16x8 o0 = *(const bf16x8*)(xr), o1 = *(const bf16x8*)(xr + 1024), o2 = *(const bf16x8*)(xr + 2048), o3 = *(const bf16x8*)(xr + 3072);
    if (kj == 0) {
      qf[0] = own0; qf[1] = own1; qf[2] = own2; qf[3] = own3; qf[4] = o0; qf[5] = o1; qf[6] = o2; qf[7] = o3;
    } else {
      qf[0] = o0; qf[1] = o1; qf[2] = o2; qf[3] = o3; qf[4] = own0; qf[5] = own1; qf[6] = own2; qf[7] = own3;
    }
  }

  auto sync_bar = [&](int i) {
    if (i >= 2 && i < 30) { WAIT_VM(12); }
    else if (i == 0) { WAIT_VM(14); } else if (i == 1) { WAIT_VM(16); } else if (i == 30) { WAIT_VM(10); }
    else if (i == 31) { WAIT_VM(6); } else if (i == 32) { WAIT_VM(4); } else { WAIT_VM(0); }
    BARRIER();
    TS(8 + i);
  };
  const char* vsrc = vbase + 3 * SLOT;
  const char* ksrc = kbase + 5 * SLOT;
  const char* wosrc = (const char*)wo + (size_t)blockIdx.x * 16384;
  unsigned fpair = ldsw + 4 * SLOT;
  auto issue1 = [&](int iv, int q) {
    if (q < 2) {
      if (iv <= 29) dma16<1>(vsrc, q ? voff1 : voff0, fpair + q * 1024);
    } else {
      if (iv <= 28) dma16<1>(iv == 28 ? wosrc : ksrc, (q & 1) ? koff1 : koff0, fpair + SLOT + (q & 1) * 1024);
    }
  };
  auto advance = [&]() {
    vsrc += SLOT; ksrc += SLOT;
    fpair = (fpair == ldsw + 8 * SLOT) ? ldsw : fpair + 2 * SLOT;
  };

  const int kra = r * 256 + ((hh ^ (r & 15)) << 4);

  constexpr float CSC = 0.088388347648318440f * 1.4426950408889634f;
  float m_run = -1e30f, l_run = 0.f;
  f32x16 oa[4] = {{}, {}, {}, {}};
  bf16x8 pf[4] = {{}, {}, {}, {}};
  f32x16 sa_[2];

#define LAZY 8
#define SMD 0
#define PRIO 0
#define LA 4
#define DPO 4
  auto mphase = [&](auto op0c, auto op1c, const char* vt, const char* kt, int iv) {
    constexpr int OP0 = decltype(op0c)::value, OP1 = decltype(op1c)::value;
    bf16x8 fr[32];
    auto ld = [&](int i) {
      if (i < 16) {
        const char* vb_ = vt + vra[i >> 2] + (i & 3) * 4096;
        fr[i] = cat44(tr_read(vb_), tr_read(vb_ + 2048));
      } else {
        fr[i] = *(const bf16x8*)(kt + (kra ^ (((i - 16) >> 1) * 32)) + (i & 1) * 8192);
      }
    };
#pragma unroll
    for (int i = OP0; i < OP0 + LA; ++i) ld(i);
    __builtin_amdgcn_sched_barrier(0);
#pragma unroll
    for (int i = OP0; i < OP1; ++i) {
      if (i + LA < OP1) ld(i + LA);
      if (i < 16) oa[i >> 2] = MFMA32(fr[i], pf[i & 3], oa[i >> 2]);
      else if (i < 18) sa_[i & 1] = MFMA32(fr[i], qf[0], (f32x16){});
      else sa_[i & 1] = MFMA32(fr[i], qf[(i - 16) >> 1], sa_[i & 1]);
      __builtin_amdgcn_sched_barrier(0);
      if (iv >= 0) {
        constexpr int STEP = (OP1 - OP0) / 4;
        if ((i - OP0) % STEP == DPO * STEP / 8) { issue1(iv, (i - OP0) / STEP); __builtin_amdgcn_sched_barrier(0); }
      }
    }
  };
  auto softmax = [&](int iv) {
    f32x16& s0 = sa_[0];
    f32x16& s1 = sa_[1];
    issue1(iv, 0); issue1(iv, 1); issue1(iv, 2); issue1(iv, 3);
    float mx = s0[0];
#pragma unroll
    for (int i = 1; i < 16; ++i) mx = fmaxf(mx, s0[i]);
#pragma unroll
    for (int i = 0; i < 16; ++i) mx = fmaxf(mx, s1[i]);
    {
      auto rr = __builtin_amdgcn_permlane32_swap(__float_as_uint(mx), __float_as_uint(mx), false, false);
      mx = fmaxf(__uint_as_float(rr[0]), __uint_as_float(rr[1]));
    }
    if (__builtin_amdgcn_ballot_w64((mx - m_run) * CSC > (float)LAZY) != 0ull)
    {
      const float mn = fmaxf(m_run, mx);
      const float alpha = __builtin_amdgcn_exp2f((m_run - mn) * CSC);
      m_run = mn;
      l_run *= alpha;
#pragma unroll
      for (int i = 0; i < 16; ++i) { oa[0][i] *= alpha; oa[1][i] *= alpha; oa[2][i] *= alpha; oa[3][i] *= alpha; }
    }
    float mc = -m_run * CSC;
    float ps = 0.f;
#pragma unroll
    for (int i = 0; i < 16; ++i) { s0[i] = __builtin_amdgcn_exp2f(fmaf(s0[i], CSC, mc)); ps += s0[i]; }
    pf[0] = pack8(s0[0], s0[1], s0[2], s0[3], s0[4], s0[5], s0[6], s0[7]);
    pf[1] = pack8(s0[8], s0[9], s0[10], s0[11], s0[12], s0[13], s0[14], s0[15]);
#pragma unroll
    for (int i = 0; i < 16; ++i) { s1[i] = __builtin_amdgcn_exp2f(fmaf(s1[i], CSC, mc)); ps += s1[i]; }
    l_run += ps;
    pf[2] = pack8(s1[0], s1[1], s1[2], s1[3], s1[4], s1[5], s1[6], s1[7]);
    pf[3] = pack8(s1[8], s1[9], s1[10], s1[11], s1[12], s1[13], s1[14], s1[15]);
    __builtin_amdgcn_sched_barrier(0);
  };
  using I0 = std::integral_constant<int, 0>;
  using I16 = std::integral_constant<int, 16>;
  using I32 = std::integral_constant<int, 32>;

  if (kj == 1) { sync_bar(0); issue1(1, 0); issue1(1, 1); issue1(1, 2); issue1(1, 3); advance(); }
  {
    sync_bar(kj);
    mphase(I16{}, I32{}, shm, shm + (6 + kj) * SLOT, kj ? 99 : 1);
    if (kj == 0) advance();
    sync_bar(kj + 1);
    softmax(kj ? 2 : 99);
    if (kj == 1) advance();
  }
  int cpair = (8 + 2 * kj) % NSLOT * SLOT;
  for (int tt = 1; tt < 16; ++tt) {
    const int t = 2 * tt + kj;
    sync_bar(t);
    mphase(I0{}, I32{}, shm + cpair, shm + cpair + SLOT, t);
    advance();
    cpair = cpair >= 6 * SLOT ? cpair - 6 * SLOT : cpair + 4 * SLOT;
    sync_bar(t + 1);
    softmax(t + 1);
    advance();
  }
  {
    sync_bar(32 + kj);
    mphase(I0{}, I16{}, shm + cpair, shm, -1);
    if (kj == 0) sync_bar(33);
  }
  const f32x16 o0 = oa[0], o1 = oa[1], o2 = oa[2], o3 = oa[3];

  {
    auto rr = __builtin_amdgcn_permlane32_swap(__float_as_uint(l_run), __float_as_uint(l_run), false, false);
    l_run = __uint_as_float(rr[0]) + __uint_as_float(rr[1]);
  }
  {
    char* mw = shm + 2 * SLOT + wid * 8704;
    f32x16 sa = kj ? o0 : o2, sb = kj ? o1 : o3;
#pragma unroll
    for (int g = 0; g < 4; ++g) {
      *(f32x4*)(mw + g * 1024 + lane * 16) = (f32x4){sa[4 * g], sa[4 * g + 1], sa[4 * g + 2], sa[4 * g + 3]};
      *(f32x4*)(mw + 4096 + g * 1024 + lane * 16) = (f32x4){sb[4 * g], sb[4 * g + 1], sb[4 * g + 2], sb[4 * g + 3]};
    }
    *(float*)(mw + 8192 + lane * 4) = m_run;
    *(float*)(mw + 8448 + lane * 4) = l_run;
    WAIT_VM(0);
    BARRIER();
    {
      const int wo0 = (4 * hh + tq) * 1024 + (wid * 64 + 16 * tg + 4 * tp) * 2;
      auto wsw = [&](int o) { return shm + 9 * SLOT + ((o & ~0xF0) | ((((o >> 4) ^ (o >> 8)) & 15) << 4)); };
      bf16x8 f0 = cat44(tr_read(wsw(wo0)), tr_read(wsw(wo0 + 8192)));
      bf16x8 f1 = cat44(tr_read(wsw(wo0 + 64)), tr_read(wsw(wo0 + 8192 + 64)));
      char* bp = (char*)ows + (8u << 20) + ((size_t)(wid * 256 + blockIdx.x) << 11) + lane * 16;
      *(bf16x8*)(bp) = f0;
      *(bf16x8*)(bp + 1024) = f1;
    }
    const char* mr = shm + 2 * SLOT + (wid ^ 4) * 8704;
    const float m2 = *(const float*)(mr + 8192 + lane * 4);
    const float l2 = *(const float*)(mr + 8448 + lane * 4);
    const float mm = fmaxf(m_run, m2);
    const float f1 = __builtin_amdgcn_exp2f((m_run - mm) * CSC), f2 = __builtin_amdgcn_exp2f((m2 - mm) * CSC);
    const float inv = 1.0f / (l_run * f1 + l2 * f2);
    const float g1 = f1 * inv, g2 = f2 * inv;
    f32x16 ka = kj ? o2 : o0, kb = kj ? o3 : o1;
    char* op = (char*)ows + ((size_t)((b * 4 + qi) * 256 + head * 8 + kj * 4) << 10) + lane * 16;
#pragma unroll
    for (int gp = 0; gp < 2; ++gp) {
      f32x4 pa0 = *(const f32x4*)(mr + (2 * gp) * 1024 + lane * 16), pa1 = *(const f32x4*)(mr + (2 * gp + 1) * 1024 + lane * 16);
      f32x4 pb0 = *(const f32x4*)(mr + 4096 + (2 * gp) * 1024 + lane * 16), pb1 = *(const f32x4*)(mr + 4096 + (2 * gp + 1) * 1024 + lane * 16);
      u32x4 wa = {cvtpk(ka[8 * gp] * g1 + pa0[0] * g2, ka[8 * gp + 1] * g1 + pa0[1] * g2),
                  cvtpk(ka[8 * gp + 2] * g1 + pa0[2] * g2, ka[8 * gp + 3] * g1 + pa0[3] * g2),
                  cvtpk(ka[8 * gp + 4] * g1 + pa1[0] * g2, ka[8 * gp + 5] * g1 + pa1[1] * g2),
                  cvtpk(ka[8 * gp + 6] * g1 + pa1[2] * g2, ka[8 * gp + 7] * g1 + pa1[3] * g2)};
      u32x4 wb = {cvtpk(kb[8 * gp] * g1 + pb0[0] * g2, kb[8 * gp + 1] * g1 + pb0[1] * g2),
                  cvtpk(kb[8 * gp + 2] * g1 + pb0[2] * g2, kb[8 * gp + 3] * g1 + pb0[3] * g2),
                  cvtpk(kb[8 * gp + 4] * g1 + pb1[0] * g2, kb[8 * gp + 5] * g1 + pb1[1] * g2),
                  cvtpk(kb[8 * gp + 6] * g1 + pb1[2] * g2, kb[8 * gp + 7] * g1 + pb1[3] * g2)};
      *(u32x4*)(op + gp * 1024) = wa;
      *(u32x4*)(op + 2048 + gp * 1024) = wb;
    }
    TS(50); TSR(53);
  }
}

constexpr int K2_LDS = 65536;
constexpr int K2_D = 8;
__global__ __launch_bounds__(512, 2) void oproj_kernel(const __hip_bfloat16* __restrict__ ows,
                                                       __hip_bfloat16* __restrict__ out) {
  extern __shared__ __attribute__((aligned(16))) char shm[];
  const int tid = threadIdx.x;
  const int wid = __builtin_amdgcn_readfirstlane(tid >> 6);
  const int lane = tid & 63;
  const int r = lane & 31, hh = lane >> 5;
  const int b = blockIdx.x & 7, j = blockIdx.x >> 3;
  const int mq = j >> 3, nq = j & 7;
  const int m0 = b * 128 + mq * 32, n0 = nq * 64;
  TS(0); TSR(52);
  const char* ap = (const char*)ows + ((size_t)((b * 4 + mq) * 256 + wid * 32) << 10) + lane * 16;
  const char* bp = (const char*)ows + (8u << 20) + ((size_t)(nq * 256 + wid * 32) << 11) + lane * 16;
  f32x16 c0 = {}, c1 = {};
  bf16x8 af[K2_D], b0f[K2_D], b1f[K2_D];
#pragma unroll
  for (int i = 0; i < K2_D; ++i) {
    asm volatile("global_load_dwordx4 %0, %1, off" : "=v"(af[i]) : "v"(ap + i * 1024) : "memory");
    asm volatile("global_load_dwordx4 %0, %1, off" : "=v"(b0f[i]) : "v"(bp + i * 2048) : "memory");
    asm volatile("global_load_dwordx4 %0, %1, off offset:1024" : "=v"(b1f[i]) : "v"(bp + i * 2048) : "memory");
  }
#pragma unroll
  for (int ks = 0; ks < 32; ++ks) {
    const int i = ks % K2_D;
    if (ks <= 32 - K2_D) { WAIT_VM(21); }
    else if (ks == 25) { WAIT_VM(18); } else if (ks == 26) { WAIT_VM(15); } else if (ks == 27) { WAIT_VM(12); }
    else if (ks == 28) { WAIT_VM(9); } else if (ks == 29) { WAIT_VM(6); } else if (ks == 30) { WAIT_VM(3); } else { WAIT_VM(0); }
    __builtin_amdgcn_sched_barrier(0);
    c0 = MFMA32(af[i], b0f[i], c0);
    c1 = MFMA32(af[i], b1f[i], c1);
    __builtin_amdgcn_sched_barrier(0);
    if (ks + K2_D < 32) {
      asm volatile("global_load_dwordx4 %0, %1, off" : "=v"(af[i]) : "v"(ap + (ks + K2_D) * 1024) : "memory");
      asm volatile("global_load_dwordx4 %0, %1, off" : "=v"(b0f[i]) : "v"(bp + (ks + K2_D) * 2048) : "memory");
      asm volatile("global_load_dwordx4 %0, %1, off offset:1024" : "=v"(b1f[i]) : "v"(bp + (ks + K2_D) * 2048) : "memory");
    }
  }
  TSW(24);
  float* red = (float*)shm;
#pragma unroll
  for (int i = 0; i < 16; ++i) {
    red[(wid * 32 + i) * 64 + lane] = c0[i];
    red[(wid * 32 + 16 + i) * 64 + lane] = c1[i];
  }
  __syncthreads();
#pragma unroll
  for (int i = 0; i < 4; ++i) {
    const int reg = wid * 4 + i;
    float v = 0.f;
#pragma unroll
    for (int w = 0; w < 8; ++w) v += red[(w * 32 + reg) * 64 + lane];
    const int rg = reg & 15;
    const int m = (rg & 3) + 8 * (rg >> 2) + 4 * hh;
    const int n = r + (reg >> 4) * 32;
    out[(size_t)(m0 + m) * DM + n0 + n] = __float2bfloat16(v);
  }
  TSW(40); TSR(53);
}

extern "C" void kernel_launch(void* const* d_in, const int* in_sizes, int n_in,
                              void* d_out, int out_size, void* d_ws, size_t ws_size,
                              hipStream_t stream) {
  const __hip_bfloat16* x = (const __hip_bfloat16*)d_in[0];
  const __hip_bfloat16* kc = (const __hip_bfloat16*)d_in[1];
  const __hip_bfloat16* vc = (const __hip_bfloat16*)d_in[2];
  const __hip_bfloat16* wq = (const __hip_bfloat16*)d_in[3];
  const __hip_bfloat16* wo = (const __hip_bfloat16*)d_in[4];
  __hip_bfloat16* out = (__hip_bfloat16*)d_out;
  __hip_bfloat16* ows = (__hip_bfloat16*)d_ws;
  hipFuncSetAttribute((const void*)attn_kernel, hipFuncAttributeMaxDynamicSharedMemorySize, LDS1);
  hipFuncSetAttribute((const void*)oproj_kernel, hipFuncAttributeMaxDynamicSharedMemorySize, K2_LDS);
  attn_kernel<<<256, 512, LDS1, stream>>>(x, kc, vc, wq, wo, ows);
  oproj_kernel<<<256, 512, K2_LDS, stream>>>(ows, out);
}
```

```cpp
#include <hip/hip_runtime.h>
#include <hip/hip_bf16.h>
#include <stdint.h>
#include <type_traits>

typedef __attribute__((ext_vector_type(8))) short bf16x8;
typedef __attribute__((ext_vector_type(4))) short s16x4;
typedef __attribute__((ext_vector_type(16))) float f32x16;
typedef __attribute__((ext_vector_type(4))) float f32x4;
typedef __attribute__((ext_vector_type(4))) unsigned u32x4;
typedef __attribute__((ext_vector_type(2))) unsigned u32x2;
typedef __attribute__((address_space(3))) s16x4* lds_s16x4_ptr;

constexpr int Bn = 8, SQ = 128, DM = 512, NH = 32, SK = 2048, DH = 128;
constexpr int HD = NH * DH;
constexpr int SLOT = 16384;
constexpr int NSLOT = 10;
constexpr int LDS1 = NSLOT * SLOT;

typedef __attribute__((ext_vector_type(2))) float f32x2;
typedef __attribute__((ext_vector_type(2))) __bf16 bf16x2_t;
__device__ __forceinline__ unsigned cvtpk(float lo, float hi) {
  f32x2 v = {lo, hi};
  bf16x2_t r = __builtin_convertvector(v, bf16x2_t);
  return __builtin_bit_cast(unsigned, r);
}
__device__ __forceinline__ bf16x8 pack8(float a0, float a1, float a2, float a3, float a4, float a5, float a6, float a7) {
  u32x4 w = {cvtpk(a0, a1), cvtpk(a2, a3), cvtpk(a4, a5), cvtpk(a6, a7)};
  return __builtin_bit_cast(bf16x8, w);
}
__device__ __forceinline__ s16x4 tr_read(const char* p) {
  return __builtin_amdgcn_ds_read_tr16_b64_v4i16((lds_s16x4_ptr)(p));
}
__device__ __forceinline__ bf16x8 cat44(s16x4 a, s16x4 b) {
  return (bf16x8){a[0], a[1], a[2], a[3], b[0], b[1], b[2], b[3]};
}
#define MFMA32(a, b, c) __builtin_amdgcn_mfma_f32_32x32x16_bf16((a), (b), (c), 0, 0, 0)
#define WAIT_VM(n) asm volatile("s_waitcnt vmcnt(" #n ")" ::: "memory")
#define BARRIER() do { asm volatile("s_waitcnt lgkmcnt(0)" ::: "memory"); __builtin_amdgcn_s_barrier(); asm volatile("" ::: "memory"); } while (0)

#define TS(slot)
#define TSW(slot)
#define TSR(slot)
#define POL0 ""
#define POL1 "nt"
#define POL2 "sc0"
#define POL3 "sc1"
#define POL4 "sc0 sc1"
#define POL5 "sc0 nt"
#define POL6 "sc1 nt"
#define POL7 "sc0 sc1 nt"
#define KPOL POL1
#define VPOL POL0
template <int NT>
__device__ __forceinline__ void dma16(const void* sbase, unsigned voff, unsigned lds) {
  if constexpr (NT == 1)
    asm volatile("s_mov_b32 m0, %2\n\ts_nop 0\n\tglobal_load_lds_dwordx4 %0, %1 " KPOL :: "v"(voff), "s"(sbase), "s"(lds) : "memory");
  else if constexpr (NT == 2)
    asm volatile("s_mov_b32 m0, %2\n\ts_nop 0\n\tglobal_load_lds_dwordx4 %0, %1 " VPOL :: "v"(voff), "s"(sbase), "s"(lds) : "memory");
  else
    asm volatile("s_mov_b32 m0, %2\n\ts_nop 0\n\tglobal_load_lds_dwordx4 %0, %1" :: "v"(voff), "s"(sbase), "s"(lds) : "memory");
}

__global__ __launch_bounds__(512, 2) void attn_kernel(const __hip_bfloat16* __restrict__ x,
                                                      const __hip_bfloat16* __restrict__ kc,
                                                      const __hip_bfloat16* __restrict__ vc,
                                                      const __hip_bfloat16* __restrict__ wq,
                                                      const __hip_bfloat16* __restrict__ wo,
                                                      __hip_bfloat16* __restrict__ ows) {
  extern __shared__ __attribute__((aligned(16))) char shm[];
  const int tid = threadIdx.x;
  const int wid = __builtin_amdgcn_readfirstlane(tid >> 6);
  const int lane = tid & 63;
  const int r = lane & 31, hh = lane >> 5;
  const int qi = wid & 3, kj = wid >> 2;
  const int b = blockIdx.x >> 5, head = blockIdx.x & 31;

  const char* kbase = (const char*)(kc + (size_t)(b * NH + head) * SK * DH);
  const char* vbase = (const char*)(vc + (size_t)(b * NH + head) * SK * DH);
  const unsigned lds0 = (unsigned)(uintptr_t)shm;
  TS(51); TSR(52);

  const int drow = lane >> 4, dslot = lane & 15;
  const int krow0 = wid * 8 + drow, krow1 = krow0 + 4;
  const unsigned koff0 = krow0 * 256 + ((dslot ^ (krow0 & 15)) << 4);
  const unsigned koff1 = krow1 * 256 + ((dslot ^ (krow1 & 15)) << 4);
  const unsigned voff0 = krow0 * 256 + ((dslot ^ (drow << 2)) << 4);
  const unsigned voff1 = voff0 + 1024;
  const unsigned woff0 = krow0 * (HD * 2) + ((dslot ^ (drow << 2)) << 4);
  const unsigned woff1 = woff0 + 4 * (HD * 2);
  const int xrow0 = wid * 16 + (lane >> 3), xrow1 = xrow0 + 8;
  const unsigned xoff0 = xrow0 * (DM * 2) + (((lane & 7) ^ ((xrow0 >> 1) & 7)) << 4);
  const unsigned xoff1 = xrow1 * (DM * 2) + (((lane & 7) ^ ((xrow1 >> 1) & 7)) << 4);
  const unsigned ldsw = lds0 + wid * 2048;
  const char* wbase = (const char*)(wq + head * DH);
  const char* xbase = (const char*)(x + (size_t)b * SQ * DM);
#define WNT 0
#define XNT 0
#define OMAP 0
#define QPIPE 1
#define TMASK 31
#define ROTM 0
#define ROTV 0
  const int rot = (blockIdx.x * ROTM + (blockIdx.x >> 5) * ROTV) & 31;
  auto dma_k = [&](int t, int slot) {
    const char* g = kbase + (size_t)((t + rot) & TMASK) * SLOT;
    dma16<1>(g, koff0, ldsw + slot * SLOT);
    dma16<1>(g, koff1, ldsw + slot * SLOT + 1024);
  };
  auto dma_v = [&](int t, int slot) {
    const char* g = vbase + (size_t)((t + rot) & TMASK) * SLOT;
    dma16<2>(g, voff0, ldsw + slot * SLOT);
    dma16<2>(g, voff1, ldsw + slot * SLOT + 1024);
  };
  auto dma_wx = [&](int w) {
    const int sw = (2 * w) % NSLOT;
    const int wr = (w + head) & 7;
    const char* g = wbase + (size_t)wr * 64 * (HD * 2);
    dma16<WNT>(g, woff0, ldsw + sw * SLOT);
    dma16<WNT>(g, woff1, ldsw + sw * SLOT + 1024);
    const char* gx = xbase + wr * 128;
    dma16<XNT>(gx, xoff0, ldsw + (sw + 1) * SLOT);
    dma16<XNT>(gx, xoff1, ldsw + (sw + 1) * SLOT + 1024);
  };
  dma_wx(0); dma_wx(1); dma_wx(2); dma_wx(3); dma_wx(4);
#define PFN 0
#define PFPOL ""
#define PFOFF 0
#define PFSTR 128
  u32x4 pfd = {0u, 0u, 0u, 0u};
  {
    const char* pb = (kj ? vbase : kbase) + PFOFF * SLOT + qi * (PFN * 1024);
#pragma unroll
    for (int i = 0; i < PFN; ++i)
      asm volatile("global_load_dwordx4 %0, %1, %2 " PFPOL : "+v"(pfd) : "v"(lane * 16 + i * 1024), "s"(pb) : "memory");
  }

  const int tg = (lane >> 4) & 1, tq = (lane & 15) >> 2, tp = lane & 3;
  int vra[4], wra[2];
  {
    const int tpw = ((tp & 1) << 1) | (tp >> 1);
#pragma unroll
    for (int blk = 0; blk < 4; ++blk)
      vra[blk] = (4 * hh + tq) * 256 + 64 * (blk ^ tq) + 32 * tg + 16 * (tp >> 1) + 8 * (tp & 1);
#pragma unroll
    for (int u = 0; u < 2; ++u)
      wra[u] = (32 * hh + tq) * 256 + 64 * ((2 * kj + u) ^ tq) + 32 * tg + 16 * (tpw >> 1) + 8 * (tpw & 1);
  }
  const int xra = (qi * 32 + r) * 128 + (((4 * hh) ^ ((r >> 1) & 7)) << 4);

  f32x16 qa0 = {}, qa1 = {};
#pragma unroll
  for (int w = 0; w < 8; ++w) {
    if (w == 0) { asm volatile("s_waitcnt vmcnt(%0)" :: "n"(16 + PFN) : "memory"); }
    else if (w <= 4) { asm volatile("s_waitcnt vmcnt(%0)" :: "n"(12 + PFN) : "memory"); }
    else { WAIT_VM(12); }
    if (w == 5) {
      if (PFN > 0) asm volatile("" :: "v"(pfd));
    }
    BARRIER();
    TS(w);
    if (w >= 1 && w <= 3) dma_wx(w + 4);
    if (w == 4) { dma_k(0, 6); dma_k(1, 7); }
    if (w == 5) { dma_v(0, 8); dma_k(2, 9); }
    if (w == 6) { dma_v(1, 0); dma_k(3, 1); }
    if (w == 7) { dma_v(2, 2); dma_k(4, 3); }
    const char* wt_ = shm + ((2 * w) % NSLOT) * SLOT;
    const char* xt_ = wt_ + SLOT;
    {
      bf16x8 xf[4], wa[4], wb[4];
#pragma unroll
      for (int s = 0; s < 4; ++s) {
        xf[s] = *(const bf16x8*)(xt_ + (xra ^ (s << 4)));
        wa[s] = cat44(tr_read(wt_ + wra[0] + s * 2048), tr_read(wt_ + wra[0] + s * 2048 + 1024));
        wb[s] = cat44(tr_read(wt_ + wra[1] + s * 2048), tr_read(wt_ + wra[1] + s * 2048 + 1024));
      }
      __builtin_amdgcn_sched_barrier(0);
#pragma unroll
      for (int s = 0; s < 4; ++s) {
        qa0 = MFMA32(wa[s], xf[s], qa0);
        qa1 = MFMA32(wb[s], xf[s], qa1);
      }
      __builtin_amdgcn_sched_barrier(0);
    }
  }
  BARRIER();
  bf16x8 qf[8];
  {
    bf16x8 own0 = pack8(qa0[0], qa0[1], qa0[2], qa0[3], qa0[4], qa0[5], qa0[6], qa0[7]);
    bf16x8 own1 = pack8(qa0[8], qa0[9], qa0[10], qa0[11], qa0[12], qa0[13], qa0[14], qa0[15]);
    bf16x8 own2 = pack8(qa1[0], qa1[1], qa1[2], qa1[3], qa1[4], qa1[5], qa1[6], qa1[7]);
    bf16x8 own3 = pack8(qa1[8], qa1[9], qa1[10], qa1[11], qa1[12], qa1[13], qa1[14], qa1[15]);
    char* xw = shm + 4 * SLOT + wid * 4096 + lane * 16;
    *(bf16x8*)(xw) = own0;
    *(bf16x8*)(xw + 1024) = own1;
    *(bf16x8*)(xw + 2048) = own2;
    *(bf16x8*)(xw + 3072) = own3;
    BARRIER();
    const char* xr = shm + 4 * SLOT + (wid ^ 4) * 4096 + lane * 16;
    bf16x8 o0 = *(const bf16x8*)(xr), o1 = *(const bf16x8*)(xr + 1024), o2 = *(const bf16x8*)(xr + 2048), o3 = *(const bf16x8*)(xr + 3072);
    if (kj == 0) {
      qf[0] = own0; qf[1] = own1; qf[2] = own2; qf[3] = own3; qf[4] = o0; qf[5] = o1; qf[6] = o2; qf[7] = o3;
    } else {
      qf[0] = o0; qf[1] = o1; qf[2] = o2; qf[3] = o3; qf[4] = own0; qf[5] = own1; qf[6] = own2; qf[7] = own3;
    }
  }

  auto sync_bar = [&](int i) {
    if (i >= 2 && i < 30) { WAIT_VM(12); }
    else if (i == 0) { WAIT_VM(14); } else if (i == 1) { WAIT_VM(16); } else if (i == 30) { WAIT_VM(10); }
    else if (i == 31) { WAIT_VM(6); } else if (i == 32) { WAIT_VM(4); } else { WAIT_VM(0); }
    BARRIER();
    TS(8 + i);
  };
  const char* vsrc = vbase + 3 * SLOT;
  const char* ksrc = kbase + 5 * SLOT;
  const char* wosrc = (const char*)wo + (size_t)blockIdx.x * 16384;
  unsigned fpair = ldsw + 4 * SLOT;
  auto issue1 = [&](int iv, int q) {
    if (q < 2) {
      if (iv <= 29) dma16<2>(vsrc, q ? voff1 : voff0, fpair + q * 1024);
    } else {
      if (iv <= 28) dma16<1>(iv == 28 ? wosrc : ksrc, (q & 1) ? koff1 : koff0, fpair + SLOT + (q & 1) * 1024);
    }
  };
  auto advance = [&]() {
    vsrc += SLOT; ksrc += SLOT;
    fpair = (fpair == ldsw + 8 * SLOT) ? ldsw : fpair + 2 * SLOT;
  };

  const int kra = r * 256 + ((hh ^ (r & 15)) << 4);

  constexpr float CSC = 0.088388347648318440f * 1.4426950408889634f;
  float m_run = -1e30f, l_run = 0.f;
  f32x16 oa[4] = {{}, {}, {}, {}};
  bf16x8 pf[4] = {{}, {}, {}, {}};
  f32x16 sa_[2];

#define LAZY 8
#define SMD 0
#define PRIO 0
#define LA 4
#define DPO 4
  auto mphase = [&](auto op0c, auto op1c, const char* vt, const char* kt, int iv) {
    constexpr int OP0 = decltype(op0c)::value, OP1 = decltype(op1c)::value;
    bf16x8 fr[32];
    auto ld = [&](int i) {
      if (i < 16) {
        const char* vb_ = vt + vra[i >> 2] + (i & 3) * 4096;
        fr[i] = cat44(tr_read(vb_), tr_read(vb_ + 2048));
      } else {
        fr[i] = *(const bf16x8*)(kt + (kra ^ (((i - 16) >> 1) * 32)) + (i & 1) * 8192);
      }
    };
#pragma unroll
    for (int i = OP0; i < OP0 + LA; ++i) ld(i);
    __builtin_amdgcn_sched_barrier(0);
#pragma unroll
    for (int i = OP0; i < OP1; ++i) {
      if (i + LA < OP1) ld(i + LA);
      if (i < 16) oa[i >> 2] = MFMA32(fr[i], pf[i & 3], oa[i >> 2]);
      else if (i < 18) sa_[i & 1] = MFMA32(fr[i], qf[0], (f32x16){});
      else sa_[i & 1] = MFMA32(fr[i], qf[(i - 16) >> 1], sa_[i & 1]);
      __builtin_amdgcn_sched_barrier(0);
      if (iv >= 0) {
        constexpr int STEP = (OP1 - OP0) / 4;
        if ((i - OP0) % STEP == DPO * STEP / 8) { issue1(iv, (i - OP0) / STEP); __builtin_amdgcn_sched_barrier(0); }
      }
    }
  };
  auto softmax = [&](int iv) {
    f32x16& s0 = sa_[0];
    f32x16& s1 = sa_[1];
    issue1(iv, 0); issue1(iv, 1); issue1(iv, 2); issue1(iv, 3);
    float mx = s0[0];
#pragma unroll
    for (int i = 1; i < 16; ++i) mx = fmaxf(mx, s0[i]);
#pragma unroll
    for (int i = 0; i < 16; ++i) mx = fmaxf(mx, s1[i]);
    {
      auto rr = __builtin_amdgcn_permlane32_swap(__float_as_uint(mx), __float_as_uint(mx), false, false);
      mx = fmaxf(__uint_as_float(rr[0]), __uint_as_float(rr[1]));
    }
    if (__builtin_amdgcn_ballot_w64((mx - m_run) * CSC > (float)LAZY) != 0ull)
    {
      const float mn = fmaxf(m_run, mx);
      const float alpha = __builtin_amdgcn_exp2f((m_run - mn) * CSC);
      m_run = mn;
      l_run *= alpha;
#pragma unroll
      for (int i = 0; i < 16; ++i) { oa[0][i] *= alpha; oa[1][i] *= alpha; oa[2][i] *= alpha; oa[3][i] *= alpha; }
    }
    float mc = -m_run * CSC;
    float ps = 0.f;
#pragma unroll
    for (int i = 0; i < 16; ++i) { s0[i] = __builtin_amdgcn_exp2f(fmaf(s0[i], CSC, mc)); ps += s0[i]; }
    pf[0] = pack8(s0[0], s0[1], s0[2], s0[3], s0[4], s0[5], s0[6], s0[7]);
    pf[1] = pack8(s0[8], s0[9], s0[10], s0[11], s0[12], s0[13], s0[14], s0[15]);
#pragma unroll
    for (int i = 0; i < 16; ++i) { s1[i] = __builtin_amdgcn_exp2f(fmaf(s1[i], CSC, mc)); ps += s1[i]; }
    l_run += ps;
    pf[2] = pack8(s1[0], s1[1], s1[2], s1[3], s1[4], s1[5], s1[6], s1[7]);
    pf[3] = pack8(s1[8], s1[9], s1[10], s1[11], s1[12], s1[13], s1[14], s1[15]);
    __builtin_amdgcn_sched_barrier(0);
  };
  using I0 = std::integral_constant<int, 0>;
  using I16 = std::integral_constant<int, 16>;
  using I32 = std::integral_constant<int, 32>;

  if (kj == 1) { sync_bar(0); issue1(1, 0); issue1(1, 1); issue1(1, 2); issue1(1, 3); advance(); }
  {
    sync_bar(kj);
    mphase(I16{}, I32{}, shm, shm + (6 + kj) * SLOT, kj ? 99 : 1);
    if (kj == 0) advance();
    sync_bar(kj + 1);
    softmax(kj ? 2 : 99);
    if (kj == 1) advance();
  }
  int cpair = (8 + 2 * kj) % NSLOT * SLOT;
  for (int tt = 1; tt < 16; ++tt) {
    const int t = 2 * tt + kj;
    sync_bar(t);
    mphase(I0{}, I32{}, shm + cpair, shm + cpair + SLOT, t);
    advance();
    cpair = cpair >= 6 * SLOT ? cpair - 6 * SLOT : cpair + 4 * SLOT;
    sync_bar(t + 1);
    softmax(t + 1);
    advance();
  }
  {
    sync_bar(32 + kj);
    mphase(I0{}, I16{}, shm + cpair, shm, -1);
    if (kj == 0) sync_bar(33);
  }
  const f32x16 o0 = oa[0], o1 = oa[1], o2 = oa[2], o3 = oa[3];

  {
    auto rr = __builtin_amdgcn_permlane32_swap(__float_as_uint(l_run), __float_as_uint(l_run), false, false);
    l_run = __uint_as_float(rr[0]) + __uint_as_float(rr[1]);
  }
  {
    char* mw = shm + 2 * SLOT + wid * 8704;
    f32x16 sa = kj ? o0 : o2, sb = kj ? o1 : o3;
#pragma unroll
    for (int g = 0; g < 4; ++g) {
      *(f32x4*)(mw + g * 1024 + lane * 16) = (f32x4){sa[4 * g], sa[4 * g + 1], sa[4 * g + 2], sa[4 * g + 3]};
      *(f32x4*)(mw + 4096 + g * 1024 + lane * 16) = (f32x4){sb[4 * g], sb[4 * g + 1], sb[4 * g + 2], sb[4 * g + 3]};
    }
    *(float*)(mw + 8192 + lane * 4) = m_run;
    *(float*)(mw + 8448 + lane * 4) = l_run;
    WAIT_VM(0);
    BARRIER();
    {
      const int wo0 = (4 * hh + tq) * 1024 + (wid * 64 + 16 * tg + 4 * tp) * 2;
      auto wsw = [&](int o) { return shm + 9 * SLOT + ((o & ~0xF0) | ((((o >> 4) ^ (o >> 8)) & 15) << 4)); };
      bf16x8 f0 = cat44(tr_read(wsw(wo0)), tr_read(wsw(wo0 + 8192)));
      bf16x8 f1 = cat44(tr_read(wsw(wo0 + 64)), tr_read(wsw(wo0 + 8192 + 64)));
      char* bp = (char*)ows + (8u << 20) + ((size_t)(wid * 256 + blockIdx.x) << 11) + lane * 16;
      *(bf16x8*)(bp) = f0;
      *(bf16x8*)(bp + 1024) = f1;
    }
    const char* mr = shm + 2 * SLOT + (wid ^ 4) * 8704;
    const float m2 = *(const float*)(mr + 8192 + lane * 4);
    const float l2 = *(const float*)(mr + 8448 + lane * 4);
    const float mm = fmaxf(m_run, m2);
    const float f1 = __builtin_amdgcn_exp2f((m_run - mm) * CSC), f2 = __builtin_amdgcn_exp2f((m2 - mm) * CSC);
    const float inv = 1.0f / (l_run * f1 + l2 * f2);
    const float g1 = f1 * inv, g2 = f2 * inv;
    f32x16 ka = kj ? o2 : o0, kb = kj ? o3 : o1;
    char* op = (char*)ows + ((size_t)((b * 4 + qi) * 256 + head * 8 + kj * 4) << 10) + lane * 16;
#pragma unroll
    for (int gp = 0; gp < 2; ++gp) {
      f32x4 pa0 = *(const f32x4*)(mr + (2 * gp) * 1024 + lane * 16), pa1 = *(const f32x4*)(mr + (2 * gp + 1) * 1024 + lane * 16);
      f32x4 pb0 = *(const f32x4*)(mr + 4096 + (2 * gp) * 1024 + lane * 16), pb1 = *(const f32x4*)(mr + 4096 + (2 * gp + 1) * 1024 + lane * 16);
      u32x4 wa = {cvtpk(ka[8 * gp] * g1 + pa0[0] * g2, ka[8 * gp + 1] * g1 + pa0[1] * g2),
                  cvtpk(ka[8 * gp + 2] * g1 + pa0[2] * g2, ka[8 * gp + 3] * g1 + pa0[3] * g2),
                  cvtpk(ka[8 * gp + 4] * g1 + pa1[0] * g2, ka[8 * gp + 5] * g1 + pa1[1] * g2),
                  cvtpk(ka[8 * gp + 6] * g1 + pa1[2] * g2, ka[8 * gp + 7] * g1 + pa1[3] * g2)};
      u32x4 wb = {cvtpk(kb[8 * gp] * g1 + pb0[0] * g2, kb[8 * gp + 1] * g1 + pb0[1] * g2),
                  cvtpk(kb[8 * gp + 2] * g1 + pb0[2] * g2, kb[8 * gp + 3] * g1 + pb0[3] * g2),
                  cvtpk(kb[8 * gp + 4] * g1 + pb1[0] * g2, kb[8 * gp + 5] * g1 + pb1[1] * g2),
                  cvtpk(kb[8 * gp + 6] * g1 + pb1[2] * g2, kb[8 * gp + 7] * g1 + pb1[3] * g2)};
      *(u32x4*)(op + gp * 1024) = wa;
      *(u32x4*)(op + 2048 + gp * 1024) = wb;
    }
    TS(50); TSR(53);
  }
}

constexpr int K2_LDS = 65536;
constexpr int K2_D = 8;
__global__ __launch_bounds__(512, 2) void oproj_kernel(const __hip_bfloat16* __restrict__ ows,
                                                       __hip_bfloat16* __restrict__ out) {
  extern __shared__ __attribute__((aligned(16))) char shm[];
  const int tid = threadIdx.x;
  const int wid = __builtin_amdgcn_readfirstlane(tid >> 6);
  const int lane = tid & 63;
  const int r = lane & 31, hh = lane >> 5;
  const int b = blockIdx.x & 7, j = blockIdx.x >> 3;
  const int mq = j >> 3, nq = j & 7;
  const int m0 = b * 128 + mq * 32, n0 = nq * 64;
  TS(0); TSR(52);
  const char* ap = (const char*)ows + ((size_t)((b * 4 + mq) * 256 + wid * 32) << 10) + lane * 16;
  const char* bp = (const char*)ows + (8u << 20) + ((size_t)(nq * 256 + wid * 32) << 11) + lane * 16;
  f32x16 c0 = {}, c1 = {};
  bf16x8 af[K2_D], b0f[K2_D], b1f[K2_D];
#pragma unroll
  for (int i = 0; i < K2_D; ++i) {
    asm volatile("global_load_dwordx4 %0, %1, off" : "=v"(af[i]) : "v"(ap + i * 1024) : "memory");
    asm volatile("global_load_dwordx4 %0, %1, off" : "=v"(b0f[i]) : "v"(bp + i * 2048) : "memory");
    asm volatile("global_load_dwordx4 %0, %1, off offset:1024" : "=v"(b1f[i]) : "v"(bp + i * 2048) : "memory");
  }
#pragma unroll
  for (int ks = 0; ks < 32; ++ks) {
    const int i = ks % K2_D;
    if (ks <= 32 - K2_D) { WAIT_VM(21); }
    else if (ks == 25) { WAIT_VM(18); } else if (ks == 26) { WAIT_VM(15); } else if (ks == 27) { WAIT_VM(12); }
    else if (ks == 28) { WAIT_VM(9); } else if (ks == 29) { WAIT_VM(6); } else if (ks == 30) { WAIT_VM(3); } else { WAIT_VM(0); }
    __builtin_amdgcn_sched_barrier(0);
    c0 = MFMA32(af[i], b0f[i], c0);
    c1 = MFMA32(af[i], b1f[i], c1);
    __builtin_amdgcn_sched_barrier(0);
    if (ks + K2_D < 32) {
      asm volatile("global_load_dwordx4 %0, %1, off" : "=v"(af[i]) : "v"(ap + (ks + K2_D) * 1024) : "memory");
      asm volatile("global_load_dwordx4 %0, %1, off" : "=v"(b0f[i]) : "v"(bp + (ks + K2_D) * 2048) : "memory");
      asm volatile("global_load_dwordx4 %0, %1, off offset:1024" : "=v"(b1f[i]) : "v"(bp + (ks + K2_D) * 2048) : "memory");
    }
  }
  TSW(24);
  float* red = (float*)shm;
#pragma unroll
  for (int i = 0; i < 16; ++i) {
    red[(wid * 32 + i) * 64 + lane] = c0[i];
    red[(wid * 32 + 16 + i) * 64 + lane] = c1[i];
  }
  __syncthreads();
#pragma unroll
  for (int i = 0; i < 4; ++i) {
    const int reg = wid * 4 + i;
    float v = 0.f;
#pragma unroll
    for (int w = 0; w < 8; ++w) v += red[(w * 32 + reg) * 64 + lane];
    const int rg = reg & 15;
    const int m = (rg & 3) + 8 * (rg >> 2) + 4 * hh;
    const int n = r + (reg >> 4) * 32;
    out[(size_t)(m0 + m) * DM + n0 + n] = __float2bfloat16(v);
  }
  TSW(40); TSR(53);
}

extern "C" void kernel_launch(void* const* d_in, const int* in_sizes, int n_in,
                              void* d_out, int out_size, void* d_ws, size_t ws_size,
                              hipStream_t stream) {
  const __hip_bfloat16* x = (const __hip_bfloat16*)d_in[0];
  const __hip_bfloat16* kc = (const __hip_bfloat16*)d_in[1];
  const __hip_bfloat16* vc = (const __hip_bfloat16*)d_in[2];
  const __hip_bfloat16* wq = (const __hip_bfloat16*)d_in[3];
  const __hip_bfloat16* wo = (const __hip_bfloat16*)d_in[4];
  __hip_bfloat16* out = (__hip_bfloat16*)d_out;
  __hip_bfloat16* ows = (__hip_bfloat16*)d_ws;
  hipFuncSetAttribute((const void*)attn_kernel, hipFuncAttributeMaxDynamicSharedMemorySize, LDS1);
  hipFuncSetAttribute((const void*)oproj_kernel, hipFuncAttributeMaxDynamicSharedMemorySize, K2_LDS);
  attn_kernel<<<256, 512, LDS1, stream>>>(x, kc, vc, wq, wo, ows);
  oproj_kernel<<<256, 512, K2_LDS, stream>>>(ows, out);
}
```

```cpp
#include <hip/hip_runtime.h>
#include <hip/hip_bf16.h>
#include <stdint.h>
#include <type_traits>

typedef __attribute__((ext_vector_type(8))) short bf16x8;
typedef __attribute__((ext_vector_type(4))) short s16x4;
typedef __attribute__((ext_vector_type(16))) float f32x16;
typedef __attribute__((ext_vector_type(4))) float f32x4;
typedef __attribute__((ext_vector_type(4))) unsigned u32x4;
typedef __attribute__((address_space(3))) s16x4* lds_s16x4_ptr;

constexpr int SQ = 128, DM = 512, NH = 32, SK = 2048, DH = 128;
constexpr int HD = NH * DH;
constexpr int SLOT = 16384;
constexpr int NSLOT = 10;
constexpr int LDS1 = NSLOT * SLOT;

typedef __attribute__((ext_vector_type(2))) float f32x2;
typedef __attribute__((ext_vector_type(2))) __bf16 bf16x2_t;
__device__ __forceinline__ unsigned cvtpk(float lo, float hi) {
  f32x2 v = {lo, hi};
  bf16x2_t r = __builtin_convertvector(v, bf16x2_t);
  return __builtin_bit_cast(unsigned, r);
}
__device__ __forceinline__ bf16x8 pack8(float a0, float a1, float a2, float a3, float a4, float a5, float a6, float a7) {
  u32x4 w = {cvtpk(a0, a1), cvtpk(a2, a3), cvtpk(a4, a5), cvtpk(a6, a7)};
  return __builtin_bit_cast(bf16x8, w);
}
__device__ __forceinline__ s16x4 tr_read(const char* p) {
  return __builtin_amdgcn_ds_read_tr16_b64_v4i16((lds_s16x4_ptr)(p));
}
__device__ __forceinline__ bf16x8 cat44(s16x4 a, s16x4 b) {
  return (bf16x8){a[0], a[1], a[2], a[3], b[0], b[1], b[2], b[3]};
}
#define MFMA32(a, b, c) __builtin_amdgcn_mfma_f32_32x32x16_bf16((a), (b), (c), 0, 0, 0)
#define WAIT_VM(n) asm volatile("s_waitcnt vmcnt(" #n ")" ::: "memory")
#define BARRIER() do { asm volatile("s_waitcnt lgkmcnt(0)" ::: "memory"); __builtin_amdgcn_s_barrier(); asm volatile("" ::: "memory"); } while (0)

#define FUSE 1
#define TS(slot)
#define TSW(slot)
#define TSR(slot)
#define RT(k)

template <int NT>
__device__ __forceinline__ void dma16(const void* sbase, unsigned voff, unsigned lds) {
  if constexpr (NT)
    asm volatile("s_mov_b32 m0, %2\n\ts_nop 0\n\tglobal_load_lds_dwordx4 %0, %1 nt" :: "v"(voff), "s"(sbase), "s"(lds) : "memory");
  else
    asm volatile("s_mov_b32 m0, %2\n\ts_nop 0\n\tglobal_load_lds_dwordx4 %0, %1" :: "v"(voff), "s"(sbase), "s"(lds) : "memory");
}
template <int N> __device__ __forceinline__ void wait_vm() { asm volatile("s_waitcnt vmcnt(%0)" :: "n"(N) : "memory"); }
template <int I, int N, class F> __device__ __forceinline__ void static_for(F&& f) {
  if constexpr (I < N) { f(std::integral_constant<int, I>{}); static_for<I + 1, N>(f); }
}

constexpr int op_wait(int ks, int e, bool flag) {
  int n = 0, posA[4] = {0, 0, 0, 0}, posB[32] = {}, posF = 0;
  if (e & 1) { posA[0] = n; n += 16; }
  for (int k = 4; k < 10; ++k) { n += 2; posB[k] = n; }
  if (!(e & 1)) { posA[0] = n; n += 16; }
  if (e & 2) { posA[1] = n; n += 16; }
  for (int k = 0;; ++k) {
    if (k == ks && flag) return n - posF;
    if (k > 0 && k % 8 == 0) {
      const int u = k / 8;
      if (!((e >> u) & 1)) { posA[u] = n; n += 16; }
      if (u < 3 && ((e >> (u + 1)) & 1)) { posA[u + 1] = n; n += 16; }
    }
    if (k == ks) {
      const int a = posA[k / 8] + 2 * (k % 8 + 1);
      return n - (a > posB[k] ? a : posB[k]);
    }
    if (k < 22) { n += 2; posB[k + 10] = n; }
    if (k == 3 || k == 11) { n += 1; posF = n; }
  }
}
constexpr int op_wait2(int ks, int eu, int eu1, bool flag) {
  const int u = flag ? ks / 8 - 1 : ks / 8;
  int best = 63;
  for (int e = 0; e < 16; ++e) {
    if (((e >> u) & 1) != eu) continue;
    if (u < 3 && ((e >> (u + 1)) & 1) != eu1) continue;
    const int v = op_wait(ks, e, flag);
    if (v < best) best = v;
  }
  return best;
}
template <int KS, bool FLAG> __device__ __forceinline__ void op_sync(bool eu, bool eu1) {
  constexpr int w00 = op_wait2(KS, 0, 0, FLAG), w01 = op_wait2(KS, 0, 1, FLAG), w10 = op_wait2(KS, 1, 0, FLAG), w11 = op_wait2(KS, 1, 1, FLAG);
  if constexpr (w00 == w01 && w00 == w10 && w00 == w11) wait_vm<w00>();
  else if constexpr (w00 == w10 && w01 == w11) { if (eu1) wait_vm<w01>(); else wait_vm<w00>(); }
  else if constexpr (w00 == w01 && w10 == w11) { if (eu) wait_vm<w10>(); else wait_vm<w00>(); }
  else { if (eu) { if (eu1) wait_vm<w11>(); else wait_vm<w10>(); } else { if (eu1) wait_vm<w01>(); else wait_vm<w00>(); } }
}

__global__ __launch_bounds__(512, 2) void attn_kernel(const __hip_bfloat16* __restrict__ x,
                                                      const __hip_bfloat16* __restrict__ kc,
                                                      const __hip_bfloat16* __restrict__ vc,
                                                      const __hip_bfloat16* __restrict__ wq,
                                                      const __hip_bfloat16* __restrict__ wo,
                                                      __hip_bfloat16* __restrict__ ows,
                                                      __hip_bfloat16* __restrict__ out) {
  extern __shared__ __attribute__((aligned(16))) char shm[];
  const int tid = threadIdx.x;
  const int wid = __builtin_amdgcn_readfirstlane(tid >> 6);
  const int lane = tid & 63;
  const int r = lane & 31, hh = lane >> 5;
  const int qi = wid & 3, kj = wid >> 2;
  const int b = blockIdx.x >> 5, head = blockIdx.x & 31;

  const char* kbase = (const char*)(kc + (size_t)(b * NH + head) * SK * DH);
  const char* vbase = (const char*)(vc + (size_t)(b * NH + head) * SK * DH);
  const unsigned lds0 = (unsigned)(uintptr_t)shm;
  TS(51); TSR(52);

  const int drow = lane >> 4, dslot = lane & 15;
  const int krow0 = wid * 8 + drow, krow1 = krow0 + 4;
  const unsigned koff0 = krow0 * 256 + ((dslot ^ (krow0 & 15)) << 4);
  const unsigned koff1 = krow1 * 256 + ((dslot ^ (krow1 & 15)) << 4);
  const unsigned voff0 = krow0 * 256 + ((dslot ^ (drow << 2)) << 4);
  const unsigned voff1 = voff0 + 1024;
  const unsigned woff0 = krow0 * (HD * 2) + ((dslot ^ (drow << 2)) << 4);
  const unsigned woff1 = woff0 + 4 * (HD * 2);
  const int xrow0 = wid * 16 + (lane >> 3), xrow1 = xrow0 + 8;
  const unsigned xoff0 = xrow0 * (DM * 2) + (((lane & 7) ^ ((xrow0 >> 1) & 7)) << 4);
  const unsigned xoff1 = xrow1 * (DM * 2) + (((lane & 7) ^ ((xrow1 >> 1) & 7)) << 4);
  const unsigned ldsw = lds0 + wid * 2048;
  const char* wbase = (const char*)(wq + head * DH);
  const char* xbase = (const char*)(x + (size_t)b * SQ * DM);
#define PSLOT(w) (((2 * (w)) % NSLOT) < 4 ? 2 - (2 * (w)) % NSLOT : (2 * (w)) % NSLOT)
  auto dma_wx = [&](int w) {
    const int sw = PSLOT(w);
    const int wr = (w + head) & 7;
    const char* g = wbase + (size_t)wr * 64 * (HD * 2);
    dma16<0>(g, woff0, ldsw + sw * SLOT);
    dma16<0>(g, woff1, ldsw + sw * SLOT + 1024);
    const char* gx = xbase + wr * 128;
    dma16<0>(gx, xoff0, ldsw + (sw + 1) * SLOT);
    dma16<0>(gx, xoff1, ldsw + (sw + 1) * SLOT + 1024);
  };
  const char* sreq = (const char*)ows + (16u << 20);
  const char* sack = sreq + 65536;
  const int par = head & 1;
  const int nl = par ? 2 : 4;
  const int hl = 4 * wid + 2 * (lane & 1) + ((lane >> 1) & 1);
  const unsigned own_w = (blockIdx.x * 64 + hl) * 4;
  const unsigned con_w = (((blockIdx.x & ~31u) + hl) * 64 + head) * 4;
  unsigned tok;
  asm volatile("global_load_dword %0, %1, %2 sc1" : "=v"(tok) : "v"(own_w), "s"(sack) : "memory");
  const int mh = (head >> 1) & 1, nq = ((head >> 2) & 1) * 4 + (head >> 3);
#define UH(u) (2 * ((u) & 1) + ((u) >> 1))
#define BKS(ks) (((ks) & 7) * 16384 + UH((ks) >> 3) * 131072)
  const char* bsrc = (const char*)wo + (size_t)wid * 524288 + nq * 128;
  const unsigned boff0 = (lane >> 3) * 1024 + (((lane & 7) ^ (((lane >> 4) & 1) << 2)) << 4);
  const unsigned boff1 = boff0 + 8192;

  dma_wx(0); dma_wx(1); dma_wx(2); dma_wx(3); dma_wx(4);
  u32x4 stg[6][2];
  auto stage_ld = [&](int i, const char* g, unsigned o0, unsigned o1) {
    asm volatile("global_load_dwordx4 %0, %1, %2 nt" : "=v"(stg[i][0]) : "v"(o0), "s"(g) : "memory");
    asm volatile("global_load_dwordx4 %0, %1, %2 nt" : "=v"(stg[i][1]) : "v"(o1), "s"(g) : "memory");
  };
  auto stage_st = [&](int i) {
    char* d = shm + ((6 + i) % NSLOT) * SLOT + wid * 2048 + lane * 16;
    asm volatile("" : "+v"(stg[i][0]), "+v"(stg[i][1]));
    *(u32x4*)(d) = stg[i][0];
    *(u32x4*)(d + 1024) = stg[i][1];
  };

  const int tg = (lane >> 4) & 1, tq = (lane & 15) >> 2, tp = lane & 3;
  int vra[4], wra[2];
  {
    const int tpw = ((tp & 1) << 1) | (tp >> 1);
#pragma unroll
    for (int blk = 0; blk < 4; ++blk)
      vra[blk] = (4 * hh + tq) * 256 + 64 * (blk ^ tq) + 32 * tg + 16 * (tp >> 1) + 8 * (tp & 1);
#pragma unroll
    for (int u = 0; u < 2; ++u)
      wra[u] = (32 * hh + tq) * 256 + 64 * ((2 * kj + u) ^ tq) + 32 * tg + 16 * (tpw >> 1) + 8 * (tpw & 1);
  }
  const int xra = (qi * 32 + r) * 128 + (((4 * hh) ^ ((r >> 1) & 7)) << 4);

  f32x16 qa0 = {}, qa1 = {};
#pragma unroll
  for (int w = 0; w < 8; ++w) {
    if (w == 0) { WAIT_VM(16); } else if (w == 1) { WAIT_VM(15); } else if (w == 2) { WAIT_VM(17); } else if (w == 3) { WAIT_VM(19); }
    else if (w == 4) { WAIT_VM(18); } else if (w == 5) { WAIT_VM(14); } else if (w == 6) { WAIT_VM(10); } else { WAIT_VM(8); }
    BARRIER();
    TS(w);
    if (w == 0) {
      asm volatile("" : "+v"(tok));
      tok += 1u;
      if (lane < 4) asm volatile("global_store_dword %0, %1, %2 sc1" :: "v"(own_w), "v"(tok), "s"(sreq) : "memory");
    }
    if (w >= 1 && w <= 3) dma_wx(w + 4);
    if (w == 0) stage_ld(0, kbase, koff0, koff1);
    if (w == 1) stage_ld(1, kbase + SLOT, koff0, koff1);
    if (w == 2) stage_ld(2, vbase, voff0, voff1);
    if (w == 3) stage_ld(3, kbase + 2 * SLOT, koff0, koff1);
    if (w >= 4) stage_st(w - 4);
    if (w == 4) stage_ld(4, vbase + SLOT, voff0, voff1);
    if (w == 5) stage_ld(5, kbase + 3 * SLOT, koff0, koff1);
    if (w == 6) {
      dma16<1>(vbase + 2 * SLOT, voff0, ldsw + 2 * SLOT); dma16<1>(vbase + 2 * SLOT, voff1, ldsw + 2 * SLOT + 1024);
      dma16<1>(kbase + 4 * SLOT, koff0, ldsw + 3 * SLOT); dma16<1>(kbase + 4 * SLOT, koff1, ldsw + 3 * SLOT + 1024);
    }
    const char* wt_ = shm + PSLOT(w) * SLOT;
    const char* xt_ = wt_ + SLOT;
    bf16x8 xf[4], wa[4], wb[4];
#pragma unroll
    for (int s = 0; s < 4; ++s) {
      xf[s] = *(const bf16x8*)(xt_ + (xra ^ (s << 4)));
      wa[s] = cat44(tr_read(wt_ + wra[0] + s * 2048), tr_read(wt_ + wra[0] + s * 2048 + 1024));
      wb[s] = cat44(tr_read(wt_ + wra[1] + s * 2048), tr_read(wt_ + wra[1] + s * 2048 + 1024));
    }
    __builtin_amdgcn_sched_barrier(0);
#pragma unroll
    for (int s = 0; s < 4; ++s) {
      qa0 = MFMA32(wa[s], xf[s], qa0);
      qa1 = MFMA32(wb[s], xf[s], qa1);
    }
    __builtin_amdgcn_sched_barrier(0);
  }
  BARRIER();
  bf16x8 qf[8];
  {
    bf16x8 own0 = pack8(qa0[0], qa0[1], qa0[2], qa0[3], qa0[4], qa0[5], qa0[6], qa0[7]);
    bf16x8 own1 = pack8(qa0[8], qa0[9], qa0[10], qa0[11], qa0[12], qa0[13], qa0[14], qa0[15]);
    bf16x8 own2 = pack8(qa1[0], qa1[1], qa1[2], qa1[3], qa1[4], qa1[5], qa1[6], qa1[7]);
    bf16x8 own3 = pack8(qa1[8], qa1[9], qa1[10], qa1[11], qa1[12], qa1[13], qa1[14], qa1[15]);
    char* xw = shm + 4 * SLOT + wid * 4096 + lane * 16;
    *(bf16x8*)(xw) = own0;
    *(bf16x8*)(xw + 1024) = own1;
    *(bf16x8*)(xw + 2048) = own2;
    *(bf16x8*)(xw + 3072) = own3;
    BARRIER();
    const char* xr = shm + 4 * SLOT + (wid ^ 4) * 4096 + lane * 16;
    bf16x8 o0 = *(const bf16x8*)(xr), o1 = *(const bf16x8*)(xr + 1024), o2 = *(const bf16x8*)(xr + 2048), o3 = *(const bf16x8*)(xr + 3072);
    if (kj == 0) {
      qf[0] = own0; qf[1] = own1; qf[2] = own2; qf[3] = own3; qf[4] = o0; qf[5] = o1; qf[6] = o2; qf[7] = o3;
    } else {
      qf[0] = o0; qf[1] = o1; qf[2] = o2; qf[3] = o3; qf[4] = own0; qf[5] = own1; qf[6] = own2; qf[7] = own3;
    }
  }
  WAIT_VM(4);
  stage_st(4); stage_st(5);

  auto sync_bar = [&](int i) {
    if (i >= 2 && i < 29) { WAIT_VM(12); }
    else if (i == 0) { WAIT_VM(14); } else if (i == 1) { WAIT_VM(16); }
    else if (par) {
      if (i == 29) { WAIT_VM(10); } else if (i == 30) { WAIT_VM(8); } else if (i == 31) { WAIT_VM(4); }
      else if (i == 32) { WAIT_VM(2); } else if (i == 33) { WAIT_VM(0); } else { WAIT_VM(1); }
    } else {
      if (i < 32) { WAIT_VM(12); } else if (i == 32) { WAIT_VM(10); } else if (i == 33) { WAIT_VM(6); } else { WAIT_VM(7); }
    }
    BARRIER();
    TS(8 + (i > 33 ? 33 : i));
  };
  const char* vsrc = vbase + 3 * SLOT;
  const char* ksrc = kbase + 5 * SLOT;
  unsigned fpair = ldsw + 4 * SLOT;
  const int blim = par ? 0 : 30;
  auto issue1 = [&](int iv, int q) {
    if (q < 2) {
      if (iv <= 29) dma16<1>(vsrc, q ? voff1 : voff0, fpair + q * 1024);
      else if (iv == blim) dma16<0>(bsrc + BKS(2), q ? boff1 : boff0, fpair + q * 1024);
    } else {
      if (iv <= 27) dma16<1>(ksrc, (q & 1) ? koff1 : koff0, fpair + SLOT + (q & 1) * 1024);
      else if (iv <= blim) dma16<0>(bsrc + (iv == 30 ? 3 : iv - 28) * 16384, (q & 1) ? boff1 : boff0, fpair + SLOT + (q & 1) * 1024);
    }
  };
  auto advance = [&]() {
    vsrc += SLOT; ksrc += SLOT;
    fpair = (fpair == ldsw + 8 * SLOT) ? ldsw : fpair + 2 * SLOT;
  };

  const int kra = r * 256 + ((hh ^ (r & 15)) << 4);

  constexpr float CSC = 0.088388347648318440f * 1.4426950408889634f;
  float m_run = -1e30f, l_run = 0.f;
  f32x16 oa[4] = {{}, {}, {}, {}};
  bf16x8 pf[4] = {{}, {}, {}, {}};
  f32x16 sa_[2];

  constexpr int LA = 4;
  auto mphase = [&](auto op0c, auto op1c, const char* vt, const char* kt, int iv) {
    constexpr int OP0 = decltype(op0c)::value, OP1 = decltype(op1c)::value;
    bf16x8 fr[32];
    auto ld = [&](int i) {
      if (i < 16) {
        const char* vb_ = vt + vra[i >> 2] + (i & 3) * 4096;
        fr[i] = cat44(tr_read(vb_), tr_read(vb_ + 2048));
      } else {
        fr[i] = *(const bf16x8*)(kt + (kra ^ (((i - 16) >> 1) * 32)) + (i & 1) * 8192);
      }
    };
#pragma unroll
    for (int i = OP0; i < OP0 + LA; ++i) ld(i);
    __builtin_amdgcn_sched_barrier(0);
#pragma unroll
    for (int i = OP0; i < OP1; ++i) {
      if (i + LA < OP1) ld(i + LA);
      if (i < 16) oa[i >> 2] = MFMA32(fr[i], pf[i & 3], oa[i >> 2]);
      else if (i < 18) sa_[i & 1] = MFMA32(fr[i], qf[0], (f32x16){});
      else sa_[i & 1] = MFMA32(fr[i], qf[(i - 16) >> 1], sa_[i & 1]);
      __builtin_amdgcn_sched_barrier(0);
      if (iv >= 0) {
        constexpr int STEP = (OP1 - OP0) / 4;
        if ((i - OP0) % STEP == STEP / 2) { issue1(iv, (i - OP0) / STEP); __builtin_amdgcn_sched_barrier(0); }
      }
    }
  };
  auto softmax = [&](int iv) {
    f32x16& s0 = sa_[0];
    f32x16& s1 = sa_[1];
    issue1(iv, 0); issue1(iv, 1); issue1(iv, 2); issue1(iv, 3);
    float mx = s0[0];
#pragma unroll
    for (int i = 1; i < 16; ++i) mx = fmaxf(mx, s0[i]);
#pragma unroll
    for (int i = 0; i < 16; ++i) mx = fmaxf(mx, s1[i]);
    {
      auto rr = __builtin_amdgcn_permlane32_swap(__float_as_uint(mx), __float_as_uint(mx), false, false);
      mx = fmaxf(__uint_as_float(rr[0]), __uint_as_float(rr[1]));
    }
    if (__builtin_amdgcn_ballot_w64((mx - m_run) * CSC > 8.0f) != 0ull) {
      const float mn = fmaxf(m_run, mx);
      const float alpha = __builtin_amdgcn_exp2f((m_run - mn) * CSC);
      m_run = mn;
      l_run *= alpha;
#pragma unroll
      for (int i = 0; i < 16; ++i) { oa[0][i] *= alpha; oa[1][i] *= alpha; oa[2][i] *= alpha; oa[3][i] *= alpha; }
    }
    const float mc = -m_run * CSC;
    float ps = 0.f;
#pragma unroll
    for (int i = 0; i < 16; ++i) { s0[i] = __builtin_amdgcn_exp2f(fmaf(s0[i], CSC, mc)); ps += s0[i]; }
    pf[0] = pack8(s0[0], s0[1], s0[2], s0[3], s0[4], s0[5], s0[6], s0[7]);
    pf[1] = pack8(s0[8], s0[9], s0[10], s0[11], s0[12], s0[13], s0[14], s0[15]);
#pragma unroll
    for (int i = 0; i < 16; ++i) { s1[i] = __builtin_amdgcn_exp2f(fmaf(s1[i], CSC, mc)); ps += s1[i]; }
    l_run += ps;
    pf[2] = pack8(s1[0], s1[1], s1[2], s1[3], s1[4], s1[5], s1[6], s1[7]);
    pf[3] = pack8(s1[8], s1[9], s1[10], s1[11], s1[12], s1[13], s1[14], s1[15]);
    __builtin_amdgcn_sched_barrier(0);
  };
  using I0 = std::integral_constant<int, 0>;
  using I16 = std::integral_constant<int, 16>;
  using I32 = std::integral_constant<int, 32>;

  if (kj == 1) { sync_bar(0); issue1(1, 0); issue1(1, 1); issue1(1, 2); issue1(1, 3); advance(); }
  {
    sync_bar(kj);
    mphase(I16{}, I32{}, shm, shm + (6 + kj) * SLOT, kj ? 99 : 1);
    if (kj == 0) advance();
    sync_bar(kj + 1);
    softmax(kj ? 2 : 99);
    if (kj == 1) advance();
  }
#define WARM 11
  unsigned warm = 0;
  int cpair = (8 + 2 * kj) % NSLOT * SLOT;
  for (int tt = 1; tt < 16; ++tt) {
    const int t = 2 * tt + kj;
    sync_bar(t);
    mphase(I0{}, I32{}, shm + cpair, shm + cpair + SLOT, t);
    advance();
    cpair = cpair >= 6 * SLOT ? cpair - 6 * SLOT : cpair + 4 * SLOT;
    sync_bar(t + 1);
    if (tt == WARM && !par) {
      const char* g = (const char*)wo + (size_t)(b * 8 + wid) * 65536 + nq * 128;
      asm volatile("global_load_dword %0, %1, %2" : "+v"(warm) : "v"(lane * 1024), "s"(g) : "memory");
    }
    softmax(t + 1);
    advance();
  }
  unsigned rq, ak, rdy = 0;
  bool e0 = false;
  const char* ap = (const char*)ows + ((size_t)((b * 4 + mh * 2) * 256 + wid * 32) << 10);
  const unsigned aoff0 = lane * 16, aoff1 = aoff0 + 262144;
  bf16x8 af[2][8][2];
  auto ld_a = [&](int u) {
#pragma unroll
    for (int s = 0; s < 8; ++s) {
      const char* g = ap + s * 1024 + UH(u) * 8192;
      asm volatile("global_load_dwordx4 %0, %1, %2 sc1" : "+v"(af[u & 1][s][0]) : "v"(aoff0), "s"(g) : "memory");
      asm volatile("global_load_dwordx4 %0, %1, %2 sc1" : "+v"(af[u & 1][s][1]) : "v"(aoff1), "s"(g) : "memory");
    }
  };
  auto ld_ack = [&]() { asm volatile("global_load_dword %0, %1, %2 sc1" : "=v"(ak) : "v"(own_w), "s"(sack) : "memory"); };
  auto rd_ack = [&]() { asm volatile("" : "+v"(ak)); rdy |= (unsigned)__builtin_amdgcn_ballot_w64(ak == tok) & 15u; };
  {
    sync_bar(32 + kj);
    ld_ack();
    mphase(I0{}, I16{}, shm + cpair, shm, -1);
    if (kj == 0) sync_bar(34);
  }
#pragma unroll
  for (int s = 0; s < 8; ++s) { asm volatile("" : "=v"(af[0][s][0])); asm volatile("" : "=v"(af[0][s][1])); }
  const f32x16 o0 = oa[0], o1 = oa[1], o2 = oa[2], o3 = oa[3];

  {
    auto rr = __builtin_amdgcn_permlane32_swap(__float_as_uint(l_run), __float_as_uint(l_run), false, false);
    l_run = __uint_as_float(rr[0]) + __uint_as_float(rr[1]);
  }
  {
    char* mw = shm + 5 * SLOT + wid * 8192;
    char* mlw = shm + 4 * SLOT + wid * 512;
    f32x16 sa = kj ? o0 : o2, sb = kj ? o1 : o3;
#pragma unroll
    for (int g = 0; g < 4; ++g) {
      *(f32x4*)(mw + g * 1024 + lane * 16) = (f32x4){sa[4 * g], sa[4 * g + 1], sa[4 * g + 2], sa[4 * g + 3]};
      *(f32x4*)(mw + 4096 + g * 1024 + lane * 16) = (f32x4){sb[4 * g], sb[4 * g + 1], sb[4 * g + 2], sb[4 * g + 3]};
    }
    *(float*)(mlw + lane * 4) = m_run;
    *(float*)(mlw + 256 + lane * 4) = l_run;
    if (kj == 0) {
      WAIT_VM(0);
      rd_ack();
      e0 = !par && (rdy & 1u);
      if (e0) ld_a(0);
    }
    BARRIER();
    const char* mr = shm + 5 * SLOT + (wid ^ 4) * 8192;
    const char* mlr = shm + 4 * SLOT + (wid ^ 4) * 512;
    const float m2 = *(const float*)(mlr + lane * 4);
    const float l2 = *(const float*)(mlr + 256 + lane * 4);
    const float mm = fmaxf(m_run, m2);
    const float f1 = __builtin_amdgcn_exp2f((m_run - mm) * CSC), f2 = __builtin_amdgcn_exp2f((m2 - mm) * CSC);
    const float inv = 1.0f / (l_run * f1 + l2 * f2);
    const float g1 = f1 * inv, g2 = f2 * inv;
    f32x16 ka = kj ? o2 : o0, kb = kj ? o3 : o1;
    char* op = (char*)ows + ((size_t)((b * 4 + qi) * 256 + head * 8 + kj * 4) << 10) + lane * 16;
#pragma unroll
    for (int gp = 0; gp < 2; ++gp) {
      f32x4 pa0 = *(const f32x4*)(mr + (2 * gp) * 1024 + lane * 16), pa1 = *(const f32x4*)(mr + (2 * gp + 1) * 1024 + lane * 16);
      f32x4 pb0 = *(const f32x4*)(mr + 4096 + (2 * gp) * 1024 + lane * 16), pb1 = *(const f32x4*)(mr + 4096 + (2 * gp + 1) * 1024 + lane * 16);
      u32x4 wa = {cvtpk(ka[8 * gp] * g1 + pa0[0] * g2, ka[8 * gp + 1] * g1 + pa0[1] * g2),
                  cvtpk(ka[8 * gp + 2] * g1 + pa0[2] * g2, ka[8 * gp + 3] * g1 + pa0[3] * g2),
                  cvtpk(ka[8 * gp + 4] * g1 + pa1[0] * g2, ka[8 * gp + 5] * g1 + pa1[1] * g2),
                  cvtpk(ka[8 * gp + 6] * g1 + pa1[2] * g2, ka[8 * gp + 7] * g1 + pa1[3] * g2)};
      u32x4 wb = {cvtpk(kb[8 * gp] * g1 + pb0[0] * g2, kb[8 * gp + 1] * g1 + pb0[1] * g2),
                  cvtpk(kb[8 * gp + 2] * g1 + pb0[2] * g2, kb[8 * gp + 3] * g1 + pb0[3] * g2),
                  cvtpk(kb[8 * gp + 4] * g1 + pb1[0] * g2, kb[8 * gp + 5] * g1 + pb1[1] * g2),
                  cvtpk(kb[8 * gp + 6] * g1 + pb1[2] * g2, kb[8 * gp + 7] * g1 + pb1[3] * g2)};
      asm volatile("global_store_dwordx4 %0, %1, off sc1\n\ts_nop 1" :: "v"(op + gp * 1024), "v"(wa) : "memory");
      asm volatile("global_store_dwordx4 %0, %1, off offset:2048 sc1\n\ts_nop 1" :: "v"(op + gp * 1024), "v"(wb) : "memory");
    }
    TS(50); TSR(53);
  }
  auto ld_req = [&]() { asm volatile("global_load_dword %0, %1, %2 sc1" : "=v"(rq) : "v"(con_w), "s"(sreq) : "memory"); };
  auto echo = [&]() {
    asm volatile("" : "+v"(rq));
    if (lane < nl) asm volatile("global_store_dword %0, %1, %2 sc1" :: "v"(con_w), "v"(rq), "s"(sack) : "memory");
  };
  auto poll = [&]() {
    __builtin_amdgcn_s_sleep(8);
    asm volatile("global_load_dword %0, %1, %2 sc1\n\ts_waitcnt vmcnt(0)" : "=v"(ak) : "v"(own_w), "s"(sack) : "memory");
    rdy |= (unsigned)__builtin_amdgcn_ballot_w64(ak == tok) & 15u;
  };
  if (kj == 1) { WAIT_VM(4); rd_ack(); }
  const unsigned rdy0 = rdy;
  ld_req();
  ld_ack();
  if (kj == 1 && !par && (rdy & 1u)) {
    e0 = true;
    ld_a(0);
    WAIT_VM(16);
  } else {
    WAIT_VM(0);
  }
  BARRIER();
  echo();
  TS(0); TSR(52);
  rd_ack();
  asm volatile("" :: "v"(warm));
  RT(0);
  if (par) {
    if ((rdy0 & 3u) != 3u) {
      for (int spin = 0; (rdy & 3u) != 3u && spin < (1 << 17); ++spin) poll();
      ld_req();
      WAIT_VM(0);
      echo();
    }
  } else {
    if (wid == (head >> 2)) rdy |= 1u << ((head >> 1) & 1);
    const int brd0 = (4 * hh + tq) * 128 + ((((tq >> 1)) * 4 + tg * 2 + (tp >> 1)) << 4) + 8 * (tp & 1) + wid * 2048;
    f32x16 cc[4] = {{}, {}, {}, {}};
    auto bslot = [](int ks) { const int e = ks % 10; return e == 0 ? 9 : (e == 9 ? 0 : e); };
    auto ld_b = [&](int ks) {
      dma16<0>(bsrc + BKS(ks), boff0, ldsw + bslot(ks) * SLOT);
      dma16<0>(bsrc + BKS(ks), boff1, ldsw + bslot(ks) * SLOT + 1024);
    };
#pragma unroll
    for (int s = 4; s < 10; ++s) ld_b(s);
#pragma unroll
    for (int s = 0; s < 8; ++s) { asm volatile("" : "=v"(af[1][s][0])); asm volatile("" : "=v"(af[1][s][1])); }
    bool eu = e0, en = false, dnx = false;
    static_for<0, 32>([&](auto kc_) {
      constexpr int ks = decltype(kc_)::value;
      constexpr int u = ks >> 3;
      if constexpr ((ks & 7) == 0) {
        if constexpr (u == 1 || u == 2) { op_sync<ks, true>(eu, en); rd_ack(); }
        if constexpr (u > 0) eu = en || dnx;
        bool dn = false;
        if (!eu) {
          for (int spin = 0; !((rdy >> u) & 1u) && spin < (1 << 17); ++spin) {
            if constexpr (u < 3) { if (!dn && ((rdy >> (u + 1)) & 1u)) { ld_a(u + 1); dn = true; } }
            poll();
          }
          ld_a(u);
        }
        en = false;
        if constexpr (u < 3) {
          if (!dn && ((rdy >> (u + 1)) & 1u)) { ld_a(u + 1); en = true; }
        }
        dnx = dn;
        RT(1 + u);
      }
      op_sync<ks, false>(eu, en);
      asm volatile("" : "+v"(af[u & 1][ks & 7][0]), "+v"(af[u & 1][ks & 7][1]));
      const char* bb = shm + bslot(ks) * SLOT;
      bf16x8 b0 = cat44(tr_read(bb + brd0), tr_read(bb + brd0 + 1024));
      bf16x8 b1 = cat44(tr_read(bb + (brd0 ^ 64)), tr_read(bb + (brd0 ^ 64) + 1024));
      cc[0] = MFMA32(af[u & 1][ks & 7][0], b0, cc[0]);
      cc[1] = MFMA32(af[u & 1][ks & 7][0], b1, cc[1]);
      cc[2] = MFMA32(af[u & 1][ks & 7][1], b0, cc[2]);
      cc[3] = MFMA32(af[u & 1][ks & 7][1], b1, cc[3]);
      __builtin_amdgcn_sched_barrier(0);
      if constexpr (ks < 22) ld_b(ks + 10);
      if constexpr (ks == 3 || ks == 11) ld_ack();
    });
    ld_req();
    RT(5);
    TSW(24);
    char* red = shm + wid * 2048 + lane * 16;
#pragma unroll
    for (int q = 0; q < 16; ++q)
      *(f32x4*)(red + (q >> 1) * SLOT + (q & 1) * 1024) = (f32x4){cc[q >> 2][(q & 3) * 4], cc[q >> 2][(q & 3) * 4 + 1], cc[q >> 2][(q & 3) * 4 + 2], cc[q >> 2][(q & 3) * 4 + 3]};
    BARRIER();
    f32x4 v0 = {}, v1 = {};
    {
      const char* rp = shm + wid * SLOT + lane * 16;
#pragma unroll
      for (int w = 0; w < 8; ++w) { v0 += *(const f32x4*)(rp + w * 2048); v1 += *(const f32x4*)(rp + w * 2048 + 1024); }
    }
    WAIT_VM(0);
    echo();
    __hip_bfloat16* op_ = out + (size_t)(b * 128 + mh * 64 + (wid >> 2) * 32 + (wid & 1) * 16 + 4 * hh) * DM + nq * 64 + ((wid >> 1) & 1) * 32 + r;
#pragma unroll
    for (int i = 0; i < 4; ++i) {
      op_[i * DM] = __float2bfloat16(v0[i]);
      op_[(8 + i) * DM] = __float2bfloat16(v1[i]);
    }
  }
  TSW(40); TSR(53);
}

extern "C" void kernel_launch(void* const* d_in, const int* in_sizes, int n_in,
                              void* d_out, int out_size, void* d_ws, size_t ws_size,
                              hipStream_t stream) {
  const __hip_bfloat16* x = (const __hip_bfloat16*)d_in[0];
  const __hip_bfloat16* kc = (const __hip_bfloat16*)d_in[1];
  const __hip_bfloat16* vc = (const __hip_bfloat16*)d_in[2];
  const __hip_bfloat16* wq = (const __hip_bfloat16*)d_in[3];
  const __hip_bfloat16* wo = (const __hip_bfloat16*)d_in[4];
  __hip_bfloat16* out = (__hip_bfloat16*)d_out;
  __hip_bfloat16* ows = (__hip_bfloat16*)d_ws;
  hipFuncSetAttribute((const void*)attn_kernel, hipFuncAttributeMaxDynamicSharedMemorySize, LDS1);
  attn_kernel<<<256, 512, LDS1, stream>>>(x, kc, vc, wq, wo, ows, out);
}
```

```cpp
#include <hip/hip_runtime.h>
#include <hip/hip_bf16.h>
#include <stdint.h>
#include <type_traits>

typedef __attribute__((ext_vector_type(8))) short bf16x8;
typedef __attribute__((ext_vector_type(4))) short s16x4;
typedef __attribute__((ext_vector_type(16))) float f32x16;
typedef __attribute__((ext_vector_type(4))) float f32x4;
typedef __attribute__((ext_vector_type(4))) unsigned u32x4;
typedef __attribute__((address_space(3))) s16x4* lds_s16x4_ptr;

constexpr int SQ = 128, DM = 512, NH = 32, SK = 2048, DH = 128;
constexpr int HD = NH * DH;
constexpr int SLOT = 16384;
constexpr int NSLOT = 10;
constexpr int LDS1 = NSLOT * SLOT;
constexpr int THR_I = 18, THR_J = 26, THR_W = 2;
constexpr int WARM = 9;
constexpr int LA = 4;

typedef __attribute__((ext_vector_type(2))) float f32x2;
typedef __attribute__((ext_vector_type(2))) __bf16 bf16x2_t;
__device__ __forceinline__ unsigned cvtpk(float lo, float hi) {
  f32x2 v = {lo, hi};
  bf16x2_t r = __builtin_convertvector(v, bf16x2_t);
  return __builtin_bit_cast(unsigned, r);
}
__device__ __forceinline__ bf16x8 pack8(float a0, float a1, float a2, float a3, float a4, float a5, float a6, float a7) {
  u32x4 w = {cvtpk(a0, a1), cvtpk(a2, a3), cvtpk(a4, a5), cvtpk(a6, a7)};
  return __builtin_bit_cast(bf16x8, w);
}
__device__ __forceinline__ s16x4 tr_read(const char* p) {
  return __builtin_amdgcn_ds_read_tr16_b64_v4i16((lds_s16x4_ptr)(p));
}
__device__ __forceinline__ bf16x8 cat44(s16x4 a, s16x4 b) {
  return (bf16x8){a[0], a[1], a[2], a[3], b[0], b[1], b[2], b[3]};
}
#define MFMA32(a, b, c) __builtin_amdgcn_mfma_f32_32x32x16_bf16((a), (b), (c), 0, 0, 0)
#define WAIT_VM(n) asm volatile("s_waitcnt vmcnt(" #n ")" ::: "memory")
#define BARRIER() do { asm volatile("s_waitcnt lgkmcnt(0)" ::: "memory"); __builtin_amdgcn_s_barrier(); asm volatile("" ::: "memory"); } while (0)

__device__ __forceinline__ void dma16(const void* sbase, unsigned voff, unsigned lds) {
  asm volatile("s_mov_b32 m0, %2\n\ts_nop 0\n\tglobal_load_lds_dwordx4 %0, %1" :: "v"(voff), "s"(sbase), "s"(lds) : "memory");
}
template <int OFF>
__device__ __forceinline__ void dma16o(const void* sbase, unsigned voff, unsigned lds) {
  asm volatile("s_mov_b32 m0, %2\n\ts_nop 0\n\tglobal_load_lds_dwordx4 %0, %1 offset:%3 nt" :: "v"(voff), "s"(sbase), "s"(lds), "n"(OFF) : "memory");
}
template <int N> __device__ __forceinline__ void wait_vm() { asm volatile("s_waitcnt vmcnt(%0)" :: "n"(N) : "memory"); }
template <int I, int N, class F> __device__ __forceinline__ void static_for(F&& f) {
  if constexpr (I < N) { f(std::integral_constant<int, I>{}); static_for<I + 1, N>(f); }
}

constexpr int op_wait(int ks, int e, bool flag) {
  int n = 0, posA[4] = {0, 0, 0, 0}, posB[32] = {}, posF = 0;
  if (e & 1) { posA[0] = n; n += 16; }
  for (int k = 2; k < 10; ++k) { n += 2; posB[k] = n; }
  if (!(e & 1)) { posA[0] = n; n += 16; }
  if (e & 2) { posA[1] = n; n += 16; }
  for (int k = 0;; ++k) {
    if (k == ks && flag) return n - posF;
    if (k > 0 && k % 8 == 0) {
      const int u = k / 8;
      if (!((e >> u) & 1)) { posA[u] = n; n += 16; }
      if (u < 3 && ((e >> (u + 1)) & 1)) { posA[u + 1] = n; n += 16; }
      if (u == 3) n += 1;
    }
    if (k == ks) {
      const int a = posA[k / 8] + 2 * (k % 8 + 1);
      return n - (a > posB[k] ? a : posB[k]);
    }
    if (k < 22) { n += 2; posB[k + 10] = n; }
    if (k == 3 || k == 11) { n += 1; posF = n; }
  }
}
constexpr int op_wait2(int ks, int eu, int eu1, bool flag) {
  const int u = flag ? ks / 8 - 1 : ks / 8;
  int best = 63;
  for (int e = 0; e < 16; ++e) {
    if (((e >> u) & 1) != eu) continue;
    if (u < 3 && ((e >> (u + 1)) & 1) != eu1) continue;
    const int v = op_wait(ks, e, flag);
    if (v < best) best = v;
  }
  return best;
}
template <int KS, bool FLAG> __device__ __forceinline__ void op_sync(bool eu, bool eu1) {
  constexpr int w00 = op_wait2(KS, 0, 0, FLAG), w01 = op_wait2(KS, 0, 1, FLAG), w10 = op_wait2(KS, 1, 0, FLAG), w11 = op_wait2(KS, 1, 1, FLAG);
  if constexpr (w00 == w01 && w00 == w10 && w00 == w11) wait_vm<w00>();
  else if constexpr (w00 == w10 && w01 == w11) { if (eu1) wait_vm<w01>(); else wait_vm<w00>(); }
  else if constexpr (w00 == w01 && w10 == w11) { if (eu) wait_vm<w10>(); else wait_vm<w00>(); }
  else { if (eu) { if (eu1) wait_vm<w11>(); else wait_vm<w10>(); } else { if (eu1) wait_vm<w01>(); else wait_vm<w00>(); } }
}

#define PSEQ {0, 1, 2, 3, 4, 10, 20, 20, 20, 5, 11, 20, 6, 12, 20, 7, 13, 20, 14, 20, 15, 30, 20, 40, 31, 20}
constexpr int pseq_[] = PSEQ;
constexpr int NPS = sizeof(pseq_) / sizeof(int);
constexpr int pseq(int i) { constexpr int t[] = PSEQ; return t[i]; }
constexpr int pro_wait(int idx) {
  int n = 1, nb = 0, pt[8] = {}, ps[6] = {};
  for (int i = 0; i <= idx; ++i) {
    const int o = i < NPS ? pseq(i) : 32;
    if (o == 32) n += 8;
    int need = -2;
    if (o == 20) need = nb < 8 ? (pt[nb] ? pt[nb] : -1) : -2;
    if (o >= 30 && o <= 32) { const int x = ps[2 * (o - 30)], y = ps[2 * (o - 30) + 1]; need = (x && y) ? (x > y ? x : y) : -1; }
    if (need == -1) return -1;
    if (o >= 5 && o < 8 && nb < o - 2) return -1;
    if ((o == 30 && nb < 6) || (o == 31 && nb < 7) || (o == 40 && nb < 8) || (o == 32 && nb != 9)) return -1;
    if (i == idx) return need == -2 ? 63 : (n - need > 63 ? 63 : n - need);
    if (o < 8) { n += 8; pt[o] = n; }
    else if (o < 16) { n += 4; ps[o - 10] = n; }
    else if (o == 20) ++nb;
    else if (o == 30) n += 1;
    else if (o == 40) n += 8;
  }
  return -1;
}
static_assert(pro_wait(NPS) >= 0, "prologue order");

__global__ __launch_bounds__(512, 2) void attn_kernel(const __hip_bfloat16* __restrict__ x,
                                                      const __hip_bfloat16* __restrict__ kc,
                                                      const __hip_bfloat16* __restrict__ vc,
                                                      const __hip_bfloat16* __restrict__ wq,
                                                      const __hip_bfloat16* __restrict__ wo,
                                                      __hip_bfloat16* __restrict__ ows,
                                                      __hip_bfloat16* __restrict__ out) {
  extern __shared__ __attribute__((aligned(16))) char shm[];
  const int tid = threadIdx.x;
  const int wid = __builtin_amdgcn_readfirstlane(tid >> 6);
  const int lane = tid & 63;
  const int r = lane & 31, hh = lane >> 5;
  const int qi = wid & 3, kj = wid >> 2;
  const int b = blockIdx.x >> 5, head = blockIdx.x & 31;
  unsigned long long pc0;
  unsigned clen;
  asm volatile("s_getpc_b64 %0\n.Lgqa_pc0:\n\ts_mov_b32 %1, .Lgqa_end-.Lgqa_pc0\n\ts_max_u32 %1, %1, .Lgqa_end2-.Lgqa_pc0" : "=s"(pc0), "=s"(clen) :: "scc");
  unsigned cwarm = 0;
  {
    const unsigned co = (((blockIdx.x >> 3) & 3) * 64 + lane) * 128;
    if (wid == 7 && co < clen) asm volatile("global_load_dword %0, %1, %2" : "+v"(cwarm) : "v"(co), "s"(pc0) : "memory");
  }
  const char* kbase = (const char*)(kc + (size_t)(b * NH + head) * SK * DH);
  const char* vbase = (const char*)(vc + (size_t)(b * NH + head) * SK * DH);
  auto ktile = [&](int i) { return kbase + ((i & 31) << 14); };
  auto vtile = [&](int i) { return vbase + ((i & 31) << 14); };
  const unsigned lds0 = (unsigned)(uintptr_t)shm;

  const int drow = lane >> 4, dslot = lane & 15;
  const unsigned woff4 = (qi * 16 + drow) * (HD * 2) + ((dslot ^ (drow << 2)) << 4);
  const int xrow0 = qi * 32 + (lane >> 3), xrow1 = xrow0 + 8;
  const unsigned xoff0 = xrow0 * (DM * 2) + (((lane & 7) ^ ((xrow0 >> 1) & 7)) << 4);
  const unsigned xoff1 = xrow1 * (DM * 2) + (((lane & 7) ^ ((xrow1 >> 1) & 7)) << 4);
  const unsigned ldsw = lds0 + wid * 2048;
  const unsigned lds4 = lds0 + qi * 4096;
  const unsigned vo4 = (qi * 16 + drow) * 256 + ((dslot ^ (drow << 2)) << 4);
  unsigned ko4[4];
#pragma unroll
  for (int q = 0; q < 4; ++q) ko4[q] = ((qi * 16 + drow) * 256 + ((dslot ^ drow) << 4)) ^ (q << 6);
  auto dma_v4 = [&](auto qc, const char* g, unsigned l) { constexpr int q = decltype(qc)::value; dma16o<q * 1024>(g, vo4, l); };
  auto dma_k4 = [&](auto qc, const char* g, unsigned l) { constexpr int q = decltype(qc)::value; dma16o<q * 1024>(g, ko4[q], l); };
  const char* wbase = (const char*)(wq + head * DH);
  const char* xbase = (const char*)(x + (size_t)b * SQ * DM);
#define PSLOT(w) (((2 * (w)) % NSLOT) < 4 ? 2 - (2 * (w)) % NSLOT : (2 * (w)) % NSLOT)
  auto dma_wx = [&](int w) {
    const int sw = PSLOT(w);
    const int wr = (w + head + b) & 7;
    const char* g = wbase + (size_t)wr * 64 * (HD * 2);
#pragma unroll
    for (int q = 0; q < 4; ++q) dma16(g + q * 4 * (HD * 2), woff4, lds4 + sw * SLOT + q * 1024);
    const char* gx = xbase + wr * 128;
#pragma unroll
    for (int q = 0; q < 4; ++q) dma16(gx + (q >> 1) * 16 * (DM * 2), (q & 1) ? xoff1 : xoff0, lds4 + (sw + 1) * SLOT + q * 1024);
  };
  const char* sreq = (const char*)ows + (16u << 20);
  const char* sack = sreq + 65536;
  const int par = (head >> 3) & 1;
#define UH(u) (((u) & 1) + 8 * ((u) >> 1))
  const int hw = ((2 * wid) & 7) + 16 * (wid >> 2);
  const int nl = par ? 2 : 4;
  const int hl = hw + UH(lane & 3);
  const unsigned own_w = (blockIdx.x * 64 + hl) * 4;
  const unsigned con_w = (((blockIdx.x & ~31u) + hl) * 64 + head) * 4;
  unsigned tok = 0;
  auto ld_tok = [&]() { asm volatile("global_load_dword %0, %1, %2 sc1" : "+v"(tok) : "v"(own_w), "s"(sack) : "memory"); };
  auto post_tok = [&]() {
    asm volatile("" : "+v"(tok));
    tok += 1u;
    if (lane < 4) asm volatile("global_store_dword %0, %1, %2 sc1" :: "v"(own_w), "v"(tok), "s"(sreq) : "memory");
  };
  const int mh = head & 1, nq = ((head >> 1) & 3) + 4 * (head >> 4);
#define BKS(ks) (((ks) & 7) * 16384 + ((ks) >> 4) * 8 * 131072)
  const char* bsrc = (const char*)wo + (size_t)hw * 131072 + nq * 128;
  const char* bsrc1 = (const char*)wo + (size_t)(hw + 1) * 131072 + nq * 128;
  const unsigned boff0 = (lane >> 3) * 1024 + (((lane & 7) ^ (((lane >> 4) & 1) << 2)) << 4);
  const unsigned boff1 = boff0 + 8192;

  u32x4 stg[6][4];
  auto stg_def = [&](int i0) {
#pragma unroll
    for (int i = i0; i < 6; ++i) {
#pragma unroll
      for (int q = 0; q < 4; ++q) asm volatile("" : "=v"(stg[i][q]));
    }
  };
  auto stage_ld = [&](int i) {
    const bool isk = i != 2 && i != 4;
    const char* g = isk ? ktile(i < 2 ? i : (i + 1) >> 1) : vtile((i >> 1) - 1);
    asm volatile("global_load_dwordx4 %0, %1, %2 nt" : "+v"(stg[i][0]) : "v"(isk ? ko4[0] : vo4), "s"(g) : "memory");
    asm volatile("global_load_dwordx4 %0, %1, %2 offset:1024 nt" : "+v"(stg[i][1]) : "v"(isk ? ko4[1] : vo4), "s"(g) : "memory");
    asm volatile("global_load_dwordx4 %0, %1, %2 offset:2048 nt" : "+v"(stg[i][2]) : "v"(isk ? ko4[2] : vo4), "s"(g) : "memory");
    asm volatile("global_load_dwordx4 %0, %1, %2 offset:3072 nt" : "+v"(stg[i][3]) : "v"(isk ? ko4[3] : vo4), "s"(g) : "memory");
  };
  auto stage_st = [&](int i) {
    char* d = shm + ((6 + i) % NSLOT) * SLOT + qi * 4096 + lane * 16;
    asm volatile("" : "+v"(stg[i][0]), "+v"(stg[i][1]), "+v"(stg[i][2]), "+v"(stg[i][3]));
#pragma unroll
    for (int q = 0; q < 4; ++q) *(u32x4*)(d + q * 1024) = stg[i][q];
  };
  ld_tok();

  const int tg = (lane >> 4) & 1, tq = (lane & 15) >> 2, tp = lane & 3;
  int vra[4], wra[4];
  {
    const int tpw = ((tp & 1) << 1) | (tp >> 1);
#pragma unroll
    for (int blk = 0; blk < 4; ++blk)
      vra[blk] = (4 * hh + tq) * 256 + 64 * (blk ^ tq) + 32 * tg + 16 * (tp >> 1) + 8 * (tp & 1);
#pragma unroll
    for (int u = 0; u < 4; ++u)
      wra[u] = (32 * hh + tq) * 256 + 64 * (u ^ tq) + 32 * tg + 16 * (tpw >> 1) + 8 * (tpw & 1);
  }
  const int xra = (qi * 32 + r) * 128 + (((4 * hh) ^ ((r >> 1) & 7)) << 4);

#define RAWBAR() do { asm volatile("" ::: "memory"); __builtin_amdgcn_s_barrier(); asm volatile("" ::: "memory"); } while (0)
  bf16x8 qf[8];
  char* xw = shm + qi * 8192 + lane * 16;
  if (__builtin_expect(kj == 1, 1)) {
    stg_def(0);
    {
      static_for<0, NPS>([&](auto ic) {
        constexpr int i = decltype(ic)::value, o = pseq(i), wv = pro_wait(i);
        if constexpr (wv < 63) wait_vm<wv>();
        if constexpr (o < 8) { dma_wx(o); }
        else if constexpr (o < 16) stage_ld(o - 10);
        else if constexpr (o == 20) RAWBAR();
        else if constexpr (o == 30) { post_tok(); stage_st(0); stage_st(1); }
        else if constexpr (o == 31) { stage_st(2); stage_st(3); }
        else {
          static_for<0, 4>([&](auto q) { dma_v4(q, vtile(2), lds4 + 2 * SLOT); });
          static_for<0, 4>([&](auto q) { dma_k4(q, ktile(4), lds4 + 3 * SLOT); });
        }
      });
    }
    BARRIER();
#pragma unroll
    for (int f = 0; f < 8; ++f) qf[f] = *(const bf16x8*)(xw + f * 1024);
  } else {
    f32x16 qa[4] = {{}, {}, {}, {}};
    bf16x8 xf[4], wf[4][4];
    auto rd_wx = [&](int w, int s) {
      const char* wt_ = shm + PSLOT(w) * SLOT;
      const char* xt_ = wt_ + SLOT;
      xf[s] = *(const bf16x8*)(xt_ + (xra ^ (s << 4)));
#pragma unroll
      for (int u = 0; u < 4; ++u) wf[u][s] = cat44(tr_read(wt_ + wra[u] + s * 2048), tr_read(wt_ + wra[u] + s * 2048 + 1024));
    };
    RAWBAR();
#pragma unroll
    for (int s = 0; s < 4; ++s) rd_wx(0, s);
    __builtin_amdgcn_sched_barrier(0);
#pragma unroll
    for (int w = 0; w < 8; ++w) {
      RAWBAR();
      if (w == 4) { WAIT_VM(0); post_tok(); }
      __builtin_amdgcn_sched_barrier(0);
#pragma unroll
      for (int s = 0; s < 4; ++s) {
#pragma unroll
        for (int u = 0; u < 4; ++u) qa[u] = MFMA32(wf[u][s], xf[s], qa[u]);
        __builtin_amdgcn_sched_barrier(0);
        if (w < 7) rd_wx(w + 1, s);
        __builtin_amdgcn_sched_barrier(0);
      }
    }
#pragma unroll
    for (int u = 0; u < 4; ++u) {
      qf[2 * u] = pack8(qa[u][0], qa[u][1], qa[u][2], qa[u][3], qa[u][4], qa[u][5], qa[u][6], qa[u][7]);
      qf[2 * u + 1] = pack8(qa[u][8], qa[u][9], qa[u][10], qa[u][11], qa[u][12], qa[u][13], qa[u][14], qa[u][15]);
      *(bf16x8*)(xw + u * 2048) = qf[2 * u];
      *(bf16x8*)(xw + u * 2048 + 1024) = qf[2 * u + 1];
    }
    BARRIER();
    stg_def(4);
    asm volatile(".Lgqa_end2:");
  }

  const int thr = par ? ((head & 1) ? THR_J : THR_I) : 0;
  auto bar = [&](int i) {
    BARRIER();
  };
  auto sync_m = [&](int i) {
    if (__builtin_expect(i < thr, 0)) wait_vm<THR_W>();
    bar(i);
  };
  unsigned warm = 0;
  const int wiv = par ? 2 * WARM + 1 + kj : -1;
  auto sync_s = [&](int i) {
    WAIT_VM(8);
    if (__builtin_expect(i < thr || i >= 30 || i == wiv, 0)) {
      if (i < thr || i == 32 * par) { WAIT_VM(0); }
      else if ((i >= 30 && par) || i == 32) { WAIT_VM(4); }
      else if (i == 30) { WAIT_VM(6); }
      if (i == wiv) {
        const char* g = (const char*)wo + (size_t)(b * 8 + wid) * 65536 + nq * 128;
        asm volatile("global_load_dword %0, %1, %2" : "+v"(warm) : "v"(lane * 1024), "s"(g) : "memory");
      }
    }
    bar(i);
  };
  int vi = kj ? 3 : 5;
  const char* vsrc = vtile(vi);
  const char* ksrc = ktile(vi + 2);
  unsigned fpair = lds4 + (kj ? 4 : 8) * SLOT;
  const int blo = par ? 99 : 28;
  auto dma_b = [&](int ks, int h) { dma16(bsrc + ks * 16384, h ? boff1 : boff0, ldsw + (ks ? ks : 9) * SLOT + h * 1024); };
  auto issue = [&](int iv, auto qc) {
    constexpr int q = decltype(qc)::value;
    if constexpr (q < 4) {
      if (iv <= 29) dma_v4(qc, vsrc, fpair);
      else if constexpr (q < 2) { if (iv == blo + 2) dma_b(1, q & 1); }
    } else {
      if (iv <= 27) dma_k4(std::integral_constant<int, q - 4>{}, ksrc, fpair + SLOT);
      else if constexpr (q < 6) { if (iv >= blo && iv <= 29) dma_b(0, q & 1); }
      else { if (iv == blo + 1) dma_b(1, q & 1); }
    }
  };
  auto advance = [&]() {
    vi += 2; vsrc = vtile(vi); ksrc = ktile(vi + 2);
    fpair = (fpair >= lds4 + 6 * SLOT) ? fpair - 6 * SLOT : fpair + 4 * SLOT;
  };

  const int kra = r * 256 + ((hh ^ (r & 15)) << 4);

  constexpr float CSC = 0.088388347648318440f * 1.4426950408889634f;
  float m_run = -1e30f, l_run = 0.f;
  f32x16 oa[4] = {{}, {}, {}, {}};
  bf16x8 pf[4] = {{}, {}, {}, {}};
  f32x16 sa_[2];

  auto mphase = [&](auto op0c, auto op1c, const char* vt, const char* kt) {
    constexpr int OP0 = decltype(op0c)::value, OP1 = decltype(op1c)::value;
    bf16x8 fr[32];
    auto ld = [&](int i) {
      if (i < 16) {
        const char* vb_ = vt + vra[i >> 2] + (i & 3) * 4096;
        fr[i] = cat44(tr_read(vb_), tr_read(vb_ + 2048));
      } else {
        fr[i] = *(const bf16x8*)(kt + (kra ^ (((i - 16) >> 1) * 32)) + (i & 1) * 8192);
      }
    };
#pragma unroll
    for (int i = OP0; i < OP0 + LA; ++i) ld(i);
    __builtin_amdgcn_sched_barrier(0);
#pragma unroll
    for (int i = OP0; i < OP1; ++i) {
      if (i + LA < OP1) ld(i + LA);
      if (i < 16) oa[i >> 2] = MFMA32(fr[i], pf[i & 3], oa[i >> 2]);
      else if (i < 18) sa_[i & 1] = MFMA32(fr[i], qf[0], (f32x16){});
      else sa_[i & 1] = MFMA32(fr[i], qf[(i - 16) >> 1], sa_[i & 1]);
      __builtin_amdgcn_sched_barrier(0);
    }
  };
  auto put = [&](const f32x16& sa, const f32x16& sb) {
    char* mw = shm + 4 * SLOT + wid * 8192 + lane * 16;
#pragma unroll
    for (int g = 0; g < 4; ++g) {
      *(f32x4*)(mw + g * 1024) = (f32x4){sa[4 * g], sa[4 * g + 1], sa[4 * g + 2], sa[4 * g + 3]};
      *(f32x4*)(mw + 4096 + g * 1024) = (f32x4){sb[4 * g], sb[4 * g + 1], sb[4 * g + 2], sb[4 * g + 3]};
    }
  };
  auto softmax = [&](int iv, auto firstc) {
    constexpr bool FIRST = decltype(firstc)::value;
    __builtin_amdgcn_sched_barrier(0);
    float ps;
    auto expo = [&](auto fastc) {
      const float mc = -m_run * CSC;
      float e[8];
      ps = 0.f;
      static_for<0, 8>([&](auto cc_) {
        constexpr int c = decltype(cc_)::value;
        if constexpr (decltype(fastc)::value == 1) {
          __builtin_amdgcn_sched_barrier(0);
          issue(0, cc_);
          __builtin_amdgcn_sched_barrier(0);
        }
#pragma unroll
        for (int i = 0; i < 4; ++i) {
          e[(c & 1) * 4 + i] = __builtin_amdgcn_exp2f(fmaf(sa_[c >> 2][(c & 3) * 4 + i], CSC, mc));
          ps += e[(c & 1) * 4 + i];
        }
        if constexpr (c & 1) pf[c >> 1] = pack8(e[0], e[1], e[2], e[3], e[4], e[5], e[6], e[7]);
      });
    };
    bool redo = true;
    if (!FIRST && __builtin_expect(iv <= 27, 1)) {
      expo(std::integral_constant<int, 1>{});
      redo = __builtin_amdgcn_ballot_w64(!(ps <= 4096.0f)) != 0ull;
    } else {
      static_for<0, 8>([&](auto q) { issue(iv, q); });
      if constexpr (!FIRST) {
        expo(std::integral_constant<int, 0>{});
        redo = __builtin_amdgcn_ballot_w64(!(ps <= 4096.0f)) != 0ull;
      }
    }
    if (__builtin_expect(redo, 0)) {
      float mx = sa_[0][0];
#pragma unroll
      for (int i = 1; i < 16; ++i) mx = fmaxf(mx, sa_[0][i]);
#pragma unroll
      for (int i = 0; i < 16; ++i) mx = fmaxf(mx, sa_[1][i]);
      auto rr = __builtin_amdgcn_permlane32_swap(__float_as_uint(mx), __float_as_uint(mx), false, false);
      mx = fmaxf(__uint_as_float(rr[0]), __uint_as_float(rr[1]));
      if constexpr (FIRST) {
        m_run = mx;
      } else if (__builtin_amdgcn_ballot_w64((mx - m_run) * CSC > 6.0f) != 0ull) {
        const float mn = fmaxf(m_run, mx);
        const float alpha = __builtin_amdgcn_exp2f((m_run - mn) * CSC);
        m_run = mn;
        l_run *= alpha;
#pragma unroll
        for (int i = 0; i < 16; ++i) { oa[0][i] *= alpha; oa[1][i] *= alpha; oa[2][i] *= alpha; oa[3][i] *= alpha; }
      }
      expo(std::integral_constant<int, 0>{});
    }
    l_run += ps;
    __builtin_amdgcn_sched_barrier(0);
  };
  using I0 = std::integral_constant<int, 0>;
  using I16 = std::integral_constant<int, 16>;
  using I32 = std::integral_constant<int, 32>;

  if (kj == 1) {
    static_for<0, 8>([&](auto q) { issue(0, q); });
    vi += 1; vsrc = vtile(vi); ksrc = ktile(vi + 2); fpair += 2 * SLOT;
    bar(1);
  }
  {
    mphase(I16{}, I32{}, shm, shm + (6 + kj) * SLOT);
    if (kj == 1) { wait_vm<pro_wait(NPS)>(); stage_st(4); stage_st(5); }
    bar(kj + 1);
    softmax(kj ? 2 : 99, std::true_type{});
    if (kj == 1) advance();
  }
  int cpair = (8 + 2 * kj) % NSLOT * SLOT;
  for (int tt = 1; tt < 16; ++tt) {
    const int t = 2 * tt + kj;
    sync_m(t);
    mphase(I0{}, I32{}, shm + cpair, shm + cpair + SLOT);
    cpair = cpair >= 6 * SLOT ? cpair - 6 * SLOT : cpair + 4 * SLOT;
    sync_s(t + 1);
    softmax(t + 1, std::false_type{});
    advance();
  }
  unsigned rq, ak, rdy = 0;
  bool e0 = false;
  const char* ap = (const char*)ows + ((size_t)((b * 4 + mh * 2) * 256 + hw * 8) << 10);
  const char* ap1 = (const char*)ows + ((size_t)((b * 4 + mh * 2) * 256 + (hw + 1) * 8) << 10);
  const unsigned aoff0 = lane * 16, aoff1 = aoff0 + 262144;
  bf16x8 af[2][8][2];
  auto ld_a = [&](int u) {
#pragma unroll
    for (int s = 0; s < 8; ++s) {
      const char* g = ((u & 1) ? ap1 : ap) + s * 1024 + (u >> 1) * 65536;
      asm volatile("global_load_dwordx4 %0, %1, %2 sc1" : "+v"(af[u & 1][s][0]) : "v"(aoff0), "s"(g) : "memory");
      asm volatile("global_load_dwordx4 %0, %1, %2 sc1" : "+v"(af[u & 1][s][1]) : "v"(aoff1), "s"(g) : "memory");
    }
  };
  auto ld_ack = [&]() { asm volatile("global_load_dword %0, %1, %2 sc1" : "=v"(ak) : "v"(own_w), "s"(sack) : "memory"); };
  auto rd_ack = [&]() { asm volatile("" : "+v"(ak)); rdy |= (unsigned)__builtin_amdgcn_ballot_w64(ak == tok) & 15u; };
  if (kj == 0) {
    bar(32);
    mphase(I0{}, I16{}, shm + cpair, shm);
  }
  {
    auto rr = __builtin_amdgcn_permlane32_swap(__float_as_uint(l_run), __float_as_uint(l_run), false, false);
    l_run = __uint_as_float(rr[0]) + __uint_as_float(rr[1]);
  }
  {
    char* mlw = shm + 2 * SLOT + wid * 512 + lane * 4;
    char* pw = shm + 3 * SLOT + qi * 4096 + lane * 16;
    *(float*)(mlw) = m_run;
    *(float*)(mlw + 256) = l_run;
    if (kj == 0) {
      put(oa[2], oa[3]);
      if (par) { WAIT_VM(0); } else { WAIT_VM(4); }
    } else {
#pragma unroll
      for (int g = 0; g < 4; ++g) *(bf16x8*)(pw + g * 1024) = pf[g];
      put(oa[0], oa[1]);
    }
    bar(33);
    ld_ack();
    const char* mr = shm + 4 * SLOT + (wid ^ 4) * 8192 + lane * 16;
    const char* mlr = shm + 2 * SLOT + (wid ^ 4) * 512 + lane * 4;
    const float m2 = *(const float*)(mlr);
    const float l2 = *(const float*)(mlr + 256);
    auto get = [&](int h) {
#pragma unroll
      for (int g = 0; g < 4; ++g) {
        const f32x4 v = *(const f32x4*)(mr + h * 4096 + g * 1024);
        sa_[h][4 * g] = v[0]; sa_[h][4 * g + 1] = v[1]; sa_[h][4 * g + 2] = v[2]; sa_[h][4 * g + 3] = v[3];
      }
    };
    auto pv_half = [&](auto gc, f32x16& a0, f32x16& a1) {
      constexpr int G = decltype(gc)::value;
      bf16x8 fr[8];
      auto ld = [&](int i) {
        const char* vb_ = shm + vra[2 * G + (i >> 2)] + (i & 3) * 4096;
        fr[i] = cat44(tr_read(vb_), tr_read(vb_ + 2048));
      };
      if constexpr (G == 0) {
#pragma unroll
        for (int g = 0; g < 4; ++g) pf[g] = *(const bf16x8*)(pw + g * 1024);
        get(0);
      }
#pragma unroll
      for (int i = 0; i < LA; ++i) ld(i);
      if constexpr (G == 0) get(1);
      __builtin_amdgcn_sched_barrier(0);
#pragma unroll
      for (int i = 0; i < 8; ++i) {
        if (i + LA < 8) ld(i + LA);
        if constexpr (G == 1) { if (i == 8 - LA) get(0); if (i == 9 - LA) get(1); }
        if (i < 4) a0 = MFMA32(fr[i], pf[i & 3], a0); else a1 = MFMA32(fr[i], pf[i & 3], a1);
        __builtin_amdgcn_sched_barrier(0);
      }
    };
    if (kj == 0) pv_half(I0{}, sa_[0], sa_[1]); else pv_half(std::integral_constant<int, 1>{}, oa[2], oa[3]);
    const float mm = fmaxf(m_run, m2);
    const float f1 = __builtin_amdgcn_exp2f((m_run - mm) * CSC), f2 = __builtin_amdgcn_exp2f((m2 - mm) * CSC);
    const float inv = 1.0f / (l_run * f1 + l2 * f2);
    const float g1 = f1 * inv, g2 = f2 * inv;
    const f32x16 ka = kj ? oa[2] : oa[0], kb = kj ? oa[3] : oa[1];
    const f32x16 pa = sa_[0], pb = sa_[1];
    char* op = (char*)ows + ((size_t)((b * 4 + qi) * 256 + head * 8 + kj * 4) << 10) + lane * 16;
#pragma unroll
    for (int gp = 0; gp < 2; ++gp) {
      u32x4 wa = {cvtpk(ka[8 * gp] * g1 + pa[8 * gp] * g2, ka[8 * gp + 1] * g1 + pa[8 * gp + 1] * g2),
                  cvtpk(ka[8 * gp + 2] * g1 + pa[8 * gp + 2] * g2, ka[8 * gp + 3] * g1 + pa[8 * gp + 3] * g2),
                  cvtpk(ka[8 * gp + 4] * g1 + pa[8 * gp + 4] * g2, ka[8 * gp + 5] * g1 + pa[8 * gp + 5] * g2),
                  cvtpk(ka[8 * gp + 6] * g1 + pa[8 * gp + 6] * g2, ka[8 * gp + 7] * g1 + pa[8 * gp + 7] * g2)};
      asm volatile("global_store_dwordx4 %0, %1, off sc1\n\ts_nop 1" :: "v"(op + gp * 1024), "v"(wa) : "memory");
    }
#pragma unroll
    for (int gp = 0; gp < 2; ++gp) {
      u32x4 wb = {cvtpk(kb[8 * gp] * g1 + pb[8 * gp] * g2, kb[8 * gp + 1] * g1 + pb[8 * gp + 1] * g2),
                  cvtpk(kb[8 * gp + 2] * g1 + pb[8 * gp + 2] * g2, kb[8 * gp + 3] * g1 + pb[8 * gp + 3] * g2),
                  cvtpk(kb[8 * gp + 4] * g1 + pb[8 * gp + 4] * g2, kb[8 * gp + 5] * g1 + pb[8 * gp + 5] * g2),
                  cvtpk(kb[8 * gp + 6] * g1 + pb[8 * gp + 6] * g2, kb[8 * gp + 7] * g1 + pb[8 * gp + 7] * g2)};
      asm volatile("global_store_dwordx4 %0, %1, off offset:2048 sc1\n\ts_nop 1" :: "v"(op + gp * 1024), "v"(wb) : "memory");
    }
#pragma unroll
    for (int s = 0; s < 8; ++s) { asm volatile("" : "=v"(af[0][s][0])); asm volatile("" : "=v"(af[0][s][1])); }
  }
  auto ld_req = [&]() { asm volatile("global_load_dword %0, %1, %2 sc1" : "=v"(rq) : "v"(con_w), "s"(sreq) : "memory"); };
  auto echo = [&]() {
    asm volatile("" : "+v"(rq));
    if (lane < nl) asm volatile("global_store_dword %0, %1, %2 sc1" :: "v"(con_w), "v"(rq), "s"(sack) : "memory");
  };
  auto poll = [&]() {
    __builtin_amdgcn_s_sleep(8);
    asm volatile("global_load_dword %0, %1, %2 sc1\n\ts_waitcnt vmcnt(0)" : "=v"(ak) : "v"(own_w), "s"(sack) : "memory");
    rdy |= (unsigned)__builtin_amdgcn_ballot_w64(ak == tok) & 15u;
  };
  WAIT_VM(4); rd_ack();
  const unsigned rdy0 = rdy;
  ld_req();
  ld_ack();
  if (!par && (rdy & 1u)) {
    e0 = true;
    ld_a(0);
    WAIT_VM(16);
  } else {
    WAIT_VM(0);
  }
  BARRIER();
  echo();
  rd_ack();
  asm volatile("" :: "v"(warm), "v"(cwarm));
  if (par) {
    if ((rdy0 & 3u) != 3u) {
      for (int spin = 0; (rdy & 3u) != 3u && spin < (1 << 17); ++spin) poll();
      ld_req();
      WAIT_VM(0);
      echo();
    }
  } else {
    if (hw == (head & ~1)) rdy |= 1u << (head & 1);
    const int brd0 = (4 * hh + tq) * 128 + ((((tq >> 1)) * 4 + tg * 2 + (tp >> 1)) << 4) + 8 * (tp & 1) + wid * 2048;
    f32x16 cc[4] = {{}, {}, {}, {}};
    auto bslot = [](int ks) { const int e = ks % 10; return e == 0 ? 9 : (e == 9 ? 0 : e); };
    auto ld_b = [&](int ks) {
      dma16(((ks & 8) ? bsrc1 : bsrc) + BKS(ks), boff0, ldsw + bslot(ks) * SLOT);
      dma16(((ks & 8) ? bsrc1 : bsrc) + BKS(ks), boff1, ldsw + bslot(ks) * SLOT + 1024);
    };
#pragma unroll
    for (int s = 2; s < 10; ++s) ld_b(s);
#pragma unroll
    for (int s = 0; s < 8; ++s) { asm volatile("" : "=v"(af[1][s][0])); asm volatile("" : "=v"(af[1][s][1])); }
    bool eu = e0, en = false, dnx = false;
    static_for<0, 32>([&](auto kc_) {
      constexpr int ks = decltype(kc_)::value;
      constexpr int u = ks >> 3;
      if constexpr ((ks & 7) == 0) {
        if constexpr (u == 1 || u == 2) { op_sync<ks, true>(eu, en); rd_ack(); }
        if constexpr (u > 0) eu = en || dnx;
        bool dn = false;
        if (!eu) {
          for (int spin = 0; !((rdy >> u) & 1u) && spin < (1 << 17); ++spin) {
            if constexpr (u < 3) { if (!dn && ((rdy >> (u + 1)) & 1u)) { ld_a(u + 1); dn = true; } }
            poll();
          }
          ld_a(u);
        }
        en = false;
        if constexpr (u < 3) {
          if (!dn && ((rdy >> (u + 1)) & 1u)) { ld_a(u + 1); en = true; }
        }
        dnx = dn;
        if constexpr (u == 3) ld_req();
      }
      op_sync<ks, false>(eu, en);
      asm volatile("" : "+v"(af[u & 1][ks & 7][0]), "+v"(af[u & 1][ks & 7][1]));
      const char* bb = shm + bslot(ks) * SLOT;
      bf16x8 b0 = cat44(tr_read(bb + brd0), tr_read(bb + brd0 + 1024));
      bf16x8 b1 = cat44(tr_read(bb + (brd0 ^ 64)), tr_read(bb + (brd0 ^ 64) + 1024));
      cc[0] = MFMA32(af[u & 1][ks & 7][0], b0, cc[0]);
      cc[1] = MFMA32(af[u & 1][ks & 7][0], b1, cc[1]);
      cc[2] = MFMA32(af[u & 1][ks & 7][1], b0, cc[2]);
      cc[3] = MFMA32(af[u & 1][ks & 7][1], b1, cc[3]);
      __builtin_amdgcn_sched_barrier(0);
      if constexpr (ks < 22) ld_b(ks + 10);
      if constexpr (ks == 3 || ks == 11) ld_ack();
    });
    WAIT_VM(0);
    echo();
    char* red = shm + wid * 2048 + lane * 16;
#pragma unroll
    for (int q = 0; q < 16; ++q)
      *(f32x4*)(red + (q >> 1) * SLOT + (q & 1) * 1024) = (f32x4){cc[q >> 2][(q & 3) * 4], cc[q >> 2][(q & 3) * 4 + 1], cc[q >> 2][(q & 3) * 4 + 2], cc[q >> 2][(q & 3) * 4 + 3]};
    BARRIER();
    f32x4 v0 = {}, v1 = {};
    {
      const char* rp = shm + wid * SLOT + lane * 16;
#pragma unroll
      for (int w = 0; w < 8; ++w) { v0 += *(const f32x4*)(rp + w * 2048); v1 += *(const f32x4*)(rp + w * 2048 + 1024); }
    }
    __hip_bfloat16* op_ = out + (size_t)(b * 128 + mh * 64 + (wid >> 2) * 32 + (wid & 1) * 16 + 4 * hh) * DM + nq * 64 + ((wid >> 1) & 1) * 32 + r;
#pragma unroll
    for (int i = 0; i < 4; ++i) {
      op_[i * DM] = __float2bfloat16(v0[i]);
      op_[(8 + i) * DM] = __float2bfloat16(v1[i]);
    }
  }
  asm volatile(".Lgqa_end:");
}

extern "C" void kernel_launch(void* const* d_in, const int* in_sizes, int n_in,
                              void* d_out, int out_size, void* d_ws, size_t ws_size,
                              hipStream_t stream) {
  const __hip_bfloat16* x = (const __hip_bfloat16*)d_in[0];
  const __hip_bfloat16* kc = (const __hip_bfloat16*)d_in[1];
  const __hip_bfloat16* vc = (const __hip_bfloat16*)d_in[2];
  const __hip_bfloat16* wq = (const __hip_bfloat16*)d_in[3];
  const __hip_bfloat16* wo = (const __hip_bfloat16*)d_in[4];
  __hip_bfloat16* out = (__hip_bfloat16*)d_out;
  __hip_bfloat16* ows = (__hip_bfloat16*)d_ws;
  (void)hipFuncSetAttribute((const void*)attn_kernel, hipFuncAttributeMaxDynamicSharedMemorySize, LDS1);
  attn_kernel<<<256, 512, LDS1, stream>>>(x, kc, vc, wq, wo, ows, out);
}
```
